```python
import torch

def _softmax(x, axis=-1):
    return torch.softmax(x, dim=axis)
def _gelu(x):
    return torch.nn.functional.gelu(x, approximate="tanh")
def _silu(x):
    return torch.nn.functional.silu(x)
def _sigmoid(x):
    return torch.sigmoid(x)
def _layernorm(x, axis=-1, eps=1e-5):
    m = x.mean(dim=axis, keepdim=True)
    v = x.var(dim=axis, keepdim=True, unbiased=False)
    return (x - m) / torch.sqrt(v + eps)
def _scatter_add(tgt, idx, src, axis=0):
    return tgt.clone().index_add_(axis, idx, src)
def _segment_sum(src, seg, n):
    out = torch.zeros((n,) + src.shape[1:], dtype=src.dtype)
    return out.index_add_(0, seg, src)
def _pad(x, pw):
    flat = tuple(v for pair in reversed(pw) for v in pair)
    return torch.nn.functional.pad(x, flat)

kernel_name = 'standalone__gqa_block_cached__near_ridge'

def setup_inputs():

    _w = (lambda a: (lambda t: t.to(torch.bfloat16) if t.is_floating_point() else t)(torch.as_tensor(a)))
    B, Sq, D = 8, 32, 512
    Hq, Hkv, Sk, Dh = 32, 8, 2048, 128
    assert Hq % Hkv == 0, (Hq, Hkv)
    return {
        "x": _w((lambda *s: torch.randn(*s, dtype=torch.bfloat16) * 0.8)(B, Sq, D)),
        "k_cache": _w((lambda *s: torch.randn(*s, dtype=torch.bfloat16) * 0.8)(B, Hkv, Sk, Dh)),
        "v_cache": _w((lambda *s: torch.randn(*s, dtype=torch.bfloat16) * 0.05)(B, Hkv, Sk, Dh)),
        "wq": _w((lambda *s: torch.randn(*s, dtype=torch.bfloat16) * 0.8)(D, Hq * Dh) * (D ** -0.5)),
        "wo": _w((lambda *s: torch.randn(*s, dtype=torch.bfloat16) * 0.05)(Hq * Dh, D) * ((Hq * Dh) ** -0.5)),
    }

def reference(x, k_cache, v_cache, wq, wo):
    B, Sq, D = x.shape
    Hq, Dh = 32, 128
    Hkv = k_cache.shape[1]
    rep = Hq // Hkv
    q = (lambda x, *d: x.permute(*d))((x @ wq).reshape(B, Sq, Hq, Dh), 0, 2, 1, 3)
    k = (lambda x, n, axis: torch.repeat_interleave(x, n, dim=axis))(k_cache, rep, axis=1)
    v = (lambda x, n, axis: torch.repeat_interleave(x, n, dim=axis))(v_cache, rep, axis=1)
    scores = torch.einsum("bhqd,bhkd->bhqk", q, k) * (Dh ** -0.5)
    p = _softmax(scores, axis=-1)
    o = torch.einsum("bhqk,bhkd->bhqd", p, v)
    return (lambda x, *d: x.permute(*d))(o, 0, 2, 1, 3).reshape(B, Sq, Hq * Dh) @ wo
```

```cpp
#include <hip/hip_runtime.h>
#include <hip/hip_bf16.h>
typedef unsigned u32x4 __attribute__((ext_vector_type(4)));
typedef unsigned u32x2 __attribute__((ext_vector_type(2)));
typedef float f32x4 __attribute__((ext_vector_type(4)));
typedef float f32x2 __attribute__((ext_vector_type(2)));
typedef float f32x16 __attribute__((ext_vector_type(16)));
typedef __bf16 bf16x8 __attribute__((ext_vector_type(8)));
typedef __bf16 bf16x4 __attribute__((ext_vector_type(4)));
typedef __bf16 bf16x2 __attribute__((ext_vector_type(2)));
typedef unsigned short u16;
#define DI __device__ __forceinline__
DI unsigned rfl(unsigned x) { return __builtin_amdgcn_readfirstlane(x); }
DI unsigned cvtpk(float lo, float hi) { f32x2 v = {lo, hi}; bf16x2 b = __builtin_convertvector(v, bf16x2); return __builtin_bit_cast(unsigned, b); }
#define MFMA32(a, b, c) __builtin_amdgcn_mfma_f32_32x32x16_bf16((a), (b), (c), 0, 0, 0)
#define MFMA16(a, b, c) __builtin_amdgcn_mfma_f32_16x16x32_bf16((a), (b), (c), 0, 0, 0)
constexpr int NB = 8, SQ = 32, DM = 512, HQ = 32, HKV = 8, SK = 2048, DH = 128;
DI int crow(int r, int h) { return (r & 3) + 8 * (r >> 2) + 4 * h; }

__global__ __launch_bounds__(256) void qproj_kernel(const u16* __restrict__ x, const u16* __restrict__ wq, u16* __restrict__ Q) {
  const int b = blockIdx.x & 7, h = blockIdx.x >> 3;
  const int lane = threadIdx.x & 63, w = threadIdx.x >> 6, r = lane & 31, hh = lane >> 5;
  f32x16 acc = {};
  const u16* xr = x + ((size_t)b * SQ + r) * DM + 8 * hh;
  const u16* wc = wq + (size_t)(8 * hh) * (HQ * DH) + h * DH + 32 * w + r;
  for (int s = 0; s < DM / 16; ++s) {
    bf16x8 bq = *(const bf16x8*)(xr + 16 * s);
    u16 t[8];
#pragma unroll
    for (int j = 0; j < 8; ++j) t[j] = wc[(size_t)(16 * s + j) * (HQ * DH)];
    u32x4 a = {t[0] | ((unsigned)t[1] << 16), t[2] | ((unsigned)t[3] << 16), t[4] | ((unsigned)t[5] << 16), t[6] | ((unsigned)t[7] << 16)};
    acc = MFMA32(__builtin_bit_cast(bf16x8, a), bq, acc);
  }
  u16* qo = Q + (((size_t)b * HQ + h) * SQ + r) * DH + 32 * w + 4 * hh;
#pragma unroll
  for (int g = 0; g < 4; ++g) { u32x2 v = {cvtpk(acc[4 * g], acc[4 * g + 1]), cvtpk(acc[4 * g + 2], acc[4 * g + 3])}; *(u32x2*)(qo + 8 * g) = v; }
}

constexpr int TK = 64;
constexpr int NSPLIT = 4, KPS = SK / NSPLIT, NT = KPS / TK;
template <int OFF> DI bf16x4 trread(unsigned addr) { bf16x4 r; asm volatile("ds_read_b64_tr_b16 %0, %1 offset:%2" : "=v"(r) : "v"(addr), "i"(OFF) : "memory"); return r; }

__global__ __launch_bounds__(256, 1) void attn_kernel(const u16* __restrict__ Q, const u16* __restrict__ K, const u16* __restrict__ V,
                                                      float* __restrict__ PO, float* __restrict__ PML) {
  extern __shared__ __attribute__((aligned(16))) char lds[];
  const unsigned tid = threadIdx.x, lane = tid & 63u, w = rfl(tid >> 6), r = lane & 31u, hh = lane >> 5;
  const unsigned bg = blockIdx.x & 63u, sp = blockIdx.x >> 6;
  const unsigned b = bg >> 3, g = bg & 7u;
  const char* kb = (const char*)K + ((size_t)bg * SK + (size_t)sp * KPS) * DH * 2;
  const char* vb = (const char*)V + ((size_t)bg * SK + (size_t)sp * KPS) * DH * 2;
  __amdgpu_buffer_rsrc_t sk = __builtin_amdgcn_make_buffer_rsrc((void*)kb, (short)0, KPS * DH * 2, 0x00020000);
  __amdgpu_buffer_rsrc_t sv = __builtin_amdgcn_make_buffer_rsrc((void*)vb, (short)0, KPS * DH * 2, 0x00020000);
  const unsigned ldsb = (unsigned)(size_t)lds;
  unsigned vk[4];
#pragma unroll
  for (int q = 0; q < 4; ++q) vk[q] = 256u * (lane >> 4) + 16u * ((lane & 15u) ^ (4u * q + (lane >> 4))) ;
  const unsigned vv = 256u * (lane >> 4) + 16u * ((lane & 15u) ^ ((lane >> 4) << 2));
  auto dmaK = [&](int t) {
    unsigned m0 = rfl(ldsb + (t & 3) * 16384u + w * 4096u), so = rfl((unsigned)t * 16384u + w * 4096u);
    asm volatile("s_mov_b32 m0, %0\n\ts_nop 0\n\t"
                 "buffer_load_dwordx4 %1, %5, %6 offen lds\n\t"
                 "buffer_load_dwordx4 %2, %5, %6 offen offset:1024 lds\n\t"
                 "buffer_load_dwordx4 %3, %5, %6 offen offset:2048 lds\n\t"
                 "buffer_load_dwordx4 %4, %5, %6 offen offset:3072 lds"
                 :: "s"(m0), "v"(vk[0]), "v"(vk[1]), "v"(vk[2]), "v"(vk[3]), "s"(sk), "s"(so) : "memory");
  };
  auto dmaV = [&](int t) {
    unsigned m0 = rfl(ldsb + 65536u + (t & 3) * 16384u + w * 4096u), so = rfl((unsigned)t * 16384u + w * 4096u);
    asm volatile("s_mov_b32 m0, %0\n\ts_nop 0\n\t"
                 "buffer_load_dwordx4 %1, %2, %3 offen lds\n\t"
                 "buffer_load_dwordx4 %1, %2, %3 offen offset:1024 lds\n\t"
                 "buffer_load_dwordx4 %1, %2, %3 offen offset:2048 lds\n\t"
                 "buffer_load_dwordx4 %1, %2, %3 offen offset:3072 lds"
                 :: "s"(m0), "v"(vv), "s"(sv), "s"(so) : "memory");
  };
  bf16x8 qf[8];
  { const u16* qp = Q + (((size_t)b * HQ + 4 * g + w) * SQ + r) * DH + 8 * hh;
#pragma unroll
    for (int s = 0; s < 8; ++s) qf[s] = *(const bf16x8*)(qp + 16 * s); }
  asm volatile("s_waitcnt vmcnt(0)" ::: "memory");
#pragma unroll
  for (int s = 0; s < 8; ++s) asm volatile("" : "+v"(qf[s]));
  for (int t = 0; t < 4; ++t) { dmaK(t); dmaV(t); }
  unsigned ka[8];
#pragma unroll
  for (int s = 0; s < 8; ++s) ka[s] = ldsb + 256u * r + 16u * ((2u * s + hh) ^ (r & 15u));
  unsigned va[4];
  { const unsigned q = (lane >> 2) & 3u, p = lane & 3u, blk = (lane >> 4) & 1u;
#pragma unroll
    for (int db = 0; db < 4; ++db) va[db] = ldsb + 65536u + 256u * (4u * hh + q) + 64u * ((unsigned)db ^ q) + 32u * blk + 8u * p; }
  constexpr float C = 0.088388347648318447f * 1.4426950408889634f;
  f32x16 o[4] = {};
  float m = -__builtin_inff(), l = 0.f;
  for (int t = 0; t < NT; ++t) {
    const int rem = (t + 3 < NT ? 3 : NT - 1 - t);
    if (rem == 3) asm volatile("s_waitcnt vmcnt(24)\n\ts_barrier" ::: "memory");
    else if (rem == 2) asm volatile("s_waitcnt vmcnt(16)\n\ts_barrier" ::: "memory");
    else if (rem == 1) asm volatile("s_waitcnt vmcnt(8)\n\ts_barrier" ::: "memory");
    else asm volatile("s_waitcnt vmcnt(0)\n\ts_barrier" ::: "memory");
    const unsigned slot = (t & 3) * 16384u;
    f32x16 s0 = {}, s1 = {};
#pragma unroll
    for (int s = 0; s < 8; ++s) {
      bf16x8 k0 = *(const bf16x8*)(lds + (ka[s] - ldsb) + slot);
      bf16x8 k1 = *(const bf16x8*)(lds + (ka[s] - ldsb) + slot + 8192);
      s0 = MFMA32(k0, qf[s], s0);
      s1 = MFMA32(k1, qf[s], s1);
    }
    float pm = s0[0];
#pragma unroll
    for (int i = 1; i < 16; ++i) pm = fmaxf(pm, s0[i]);
#pragma unroll
    for (int i = 0; i < 16; ++i) pm = fmaxf(pm, s1[i]);
    { auto rr = __builtin_amdgcn_permlane32_swap(__float_as_uint(pm), __float_as_uint(pm), false, false);
      pm = fmaxf(__uint_as_float(rr[0]), __uint_as_float(rr[1])); }
    const float mn = fmaxf(m, pm);
    const float alpha = __builtin_amdgcn_exp2f((m - mn) * C);
    m = mn;
    const float mc = -mn * C;
    float ps = 0.f;
#pragma unroll
    for (int i = 0; i < 16; ++i) { s0[i] = __builtin_amdgcn_exp2f(fmaf(s0[i], C, mc)); ps += s0[i]; }
#pragma unroll
    for (int i = 0; i < 16; ++i) { s1[i] = __builtin_amdgcn_exp2f(fmaf(s1[i], C, mc)); ps += s1[i]; }
    l = l * alpha + ps;
#pragma unroll
    for (int db = 0; db < 4; ++db)
#pragma unroll
      for (int i = 0; i < 16; ++i) o[db][i] *= alpha;
    bf16x8 pf[4];
#pragma unroll
    for (int ks = 0; ks < 2; ++ks) {
      u32x4 a = {cvtpk(s0[8 * ks], s0[8 * ks + 1]), cvtpk(s0[8 * ks + 2], s0[8 * ks + 3]), cvtpk(s0[8 * ks + 4], s0[8 * ks + 5]), cvtpk(s0[8 * ks + 6], s0[8 * ks + 7])};
      pf[ks] = __builtin_bit_cast(bf16x8, a);
      u32x4 c = {cvtpk(s1[8 * ks], s1[8 * ks + 1]), cvtpk(s1[8 * ks + 2], s1[8 * ks + 3]), cvtpk(s1[8 * ks + 4], s1[8 * ks + 5]), cvtpk(s1[8 * ks + 6], s1[8 * ks + 7])};
      pf[2 + ks] = __builtin_bit_cast(bf16x8, c);
    }
#pragma unroll
    for (int db = 0; db < 4; ++db) {
      const unsigned a = va[db] + slot;
      bf16x4 lo0 = trread<0>(a), hi0 = trread<2048>(a), lo1 = trread<4096>(a), hi1 = trread<6144>(a);
      bf16x4 lo2 = trread<8192>(a), hi2 = trread<10240>(a), lo3 = trread<12288>(a), hi3 = trread<14336>(a);
      asm volatile("s_waitcnt lgkmcnt(0)" : "+v"(lo0), "+v"(hi0), "+v"(lo1), "+v"(hi1), "+v"(lo2), "+v"(hi2), "+v"(lo3), "+v"(hi3) :: "memory");
      __builtin_amdgcn_sched_barrier(0);
      o[db] = MFMA32(__builtin_shufflevector(lo0, hi0, 0, 1, 2, 3, 4, 5, 6, 7), pf[0], o[db]);
      o[db] = MFMA32(__builtin_shufflevector(lo1, hi1, 0, 1, 2, 3, 4, 5, 6, 7), pf[1], o[db]);
      o[db] = MFMA32(__builtin_shufflevector(lo2, hi2, 0, 1, 2, 3, 4, 5, 6, 7), pf[2], o[db]);
      o[db] = MFMA32(__builtin_shufflevector(lo3, hi3, 0, 1, 2, 3, 4, 5, 6, 7), pf[3], o[db]);
    }
    if (t + 4 < NT) {
      asm volatile("s_waitcnt lgkmcnt(0)\n\ts_barrier" ::: "memory");
      dmaK(t + 4); dmaV(t + 4);
    }
  }
  { auto rr = __builtin_amdgcn_permlane32_swap(__float_as_uint(l), __float_as_uint(l), false, false);
    l = __uint_as_float(rr[0]) + __uint_as_float(rr[1]); }
  const size_t pi = (((size_t)bg * 4 + w) * NSPLIT + sp) * SQ + r;
  if (hh == 0) { PML[pi * 2] = m * C; PML[pi * 2 + 1] = l; }
  float* po = PO + pi * DH + 4 * hh;
#pragma unroll
  for (int db = 0; db < 4; ++db)
#pragma unroll
    for (int gq = 0; gq < 4; ++gq) { f32x4 v = {o[db][4 * gq], o[db][4 * gq + 1], o[db][4 * gq + 2], o[db][4 * gq + 3]}; *(f32x4*)(po + 32 * db + 8 * gq) = v; }
}

__global__ __launch_bounds__(256) void combine_kernel(const float* __restrict__ PO, const float* __restrict__ PML, u16* __restrict__ O) {
  const int bh = blockIdx.x, row = threadIdx.x >> 3, d0 = (threadIdx.x & 7) * 16;
  const int b = bh >> 5, h = bh & 31;
  float mm[4], ll[4], M = -__builtin_inff();
#pragma unroll
  for (int s = 0; s < 4; ++s) { const size_t pi = ((size_t)bh * 4 + s) * SQ + row; mm[s] = PML[pi * 2]; ll[s] = PML[pi * 2 + 1]; M = fmaxf(M, mm[s]); }
  float L = 0.f, wt[4];
#pragma unroll
  for (int s = 0; s < 4; ++s) { wt[s] = __builtin_amdgcn_exp2f(mm[s] - M); L += wt[s] * ll[s]; }
  const float inv = 1.f / L;
  float acc[16] = {};
#pragma unroll
  for (int s = 0; s < 4; ++s) { const float* p = PO + (((size_t)bh * 4 + s) * SQ + row) * DH + d0;
#pragma unroll
    for (int i = 0; i < 4; ++i) { f32x4 v = *(const f32x4*)(p + 4 * i); acc[4 * i] += wt[s] * v[0]; acc[4 * i + 1] += wt[s] * v[1]; acc[4 * i + 2] += wt[s] * v[2]; acc[4 * i + 3] += wt[s] * v[3]; } }
  u16* op = O + ((size_t)b * SQ + row) * (HQ * DH) + h * DH + d0;
  u32x4 a = {cvtpk(acc[0] * inv, acc[1] * inv), cvtpk(acc[2] * inv, acc[3] * inv), cvtpk(acc[4] * inv, acc[5] * inv), cvtpk(acc[6] * inv, acc[7] * inv)};
  u32x4 c = {cvtpk(acc[8] * inv, acc[9] * inv), cvtpk(acc[10] * inv, acc[11] * inv), cvtpk(acc[12] * inv, acc[13] * inv), cvtpk(acc[14] * inv, acc[15] * inv)};
  *(u32x4*)op = a; *(u32x4*)(op + 8) = c;
}

__global__ __launch_bounds__(256) void oproj_kernel(const u16* __restrict__ O, const u16* __restrict__ wo, u16* __restrict__ out) {
  __shared__ float red[4][32][16];
  const int b = blockIdx.x & 7, cb = blockIdx.x >> 3;
  const int lane = threadIdx.x & 63, w = threadIdx.x >> 6, c = lane & 15, kq = lane >> 4;
  f32x4 a0 = {}, a1 = {};
  const u16* o0 = O + ((size_t)b * SQ + c) * 4096 + w * 1024 + 8 * kq;
  const u16* o1 = o0 + 16 * 4096;
  const u16* wp = wo + (size_t)(w * 1024 + 8 * kq) * DM + cb * 16 + c;
  for (int s = 0; s < 32; ++s) {
    bf16x8 x0 = *(const bf16x8*)(o0 + 32 * s), x1 = *(const bf16x8*)(o1 + 32 * s);
    u16 t[8];
#pragma unroll
    for (int j = 0; j < 8; ++j) t[j] = wp[(size_t)(32 * s + j) * DM];
    u32x4 a = {t[0] | ((unsigned)t[1] << 16), t[2] | ((unsigned)t[3] << 16), t[4] | ((unsigned)t[5] << 16), t[6] | ((unsigned)t[7] << 16)};
    bf16x8 bb = __builtin_bit_cast(bf16x8, a);
    a0 = MFMA16(x0, bb, a0); a1 = MFMA16(x1, bb, a1);
  }
#pragma unroll
  for (int i = 0; i < 4; ++i) { red[w][4 * kq + i][c] = a0[i]; red[w][16 + 4 * kq + i][c] = a1[i]; }
  __syncthreads();
  const int t = threadIdx.x;
  { const int row = t >> 3, c2 = (t & 7) * 2;
    float v0 = red[0][row][c2] + red[1][row][c2] + red[2][row][c2] + red[3][row][c2];
    float v1 = red[0][row][c2 + 1] + red[1][row][c2 + 1] + red[2][row][c2 + 1] + red[3][row][c2 + 1];
    *(unsigned*)(out + ((size_t)b * SQ + row) * DM + cb * 16 + c2) = cvtpk(v0, v1); }
}

extern "C" void kernel_launch(void* const* d_in, const int* in_sizes, int n_in, void* d_out, int out_size, void* d_ws, size_t ws_size, hipStream_t stream) {
  const u16* x = (const u16*)d_in[0]; const u16* K = (const u16*)d_in[1]; const u16* V = (const u16*)d_in[2];
  const u16* wq = (const u16*)d_in[3]; const u16* wo = (const u16*)d_in[4];
  char* ws = (char*)d_ws;
  u16* Q = (u16*)ws;
  u16* O = (u16*)(ws + (2 << 20));
  float* PML = (float*)(ws + (4 << 20));
  float* PO = (float*)(ws + (5 << 20));
  (void)hipFuncSetAttribute((const void*)attn_kernel, hipFuncAttributeMaxDynamicSharedMemorySize, 131072);
  qproj_kernel<<<256, 256, 0, stream>>>(x, wq, Q);
  attn_kernel<<<256, 256, 131072, stream>>>(Q, K, V, PO, PML);
  combine_kernel<<<256, 256, 0, stream>>>(PO, PML, O);
  oproj_kernel<<<256, 256, 0, stream>>>(O, wo, (u16*)d_out);
}
```

```cpp
#include <hip/hip_runtime.h>
#include <hip/hip_bf16.h>
typedef unsigned u32x4 __attribute__((ext_vector_type(4)));
typedef unsigned u32x2 __attribute__((ext_vector_type(2)));
typedef float f32x2 __attribute__((ext_vector_type(2)));
typedef float f32x16 __attribute__((ext_vector_type(16)));
typedef unsigned u32x16 __attribute__((ext_vector_type(16)));
typedef unsigned u32x8 __attribute__((ext_vector_type(8)));
typedef __bf16 bf16x8 __attribute__((ext_vector_type(8)));
typedef __bf16 bf16x4 __attribute__((ext_vector_type(4)));
typedef __bf16 bf16x2 __attribute__((ext_vector_type(2)));
typedef unsigned short u16;
typedef unsigned long long u64;
typedef __attribute__((address_space(1))) u64 gu64;

#define MAIN_ASM \
  "v_add_u32_e32 v244, 0x10000, v224\n\t" \
  "v_add_u32_e32 v245, 0x10000, v225\n\t" \
  "v_add_u32_e32 v246, 0x10000, v226\n\t" \
  "v_add_u32_e32 v247, 0x10000, v227\n\t" \
  "ds_read_b32 v236, v228\n\t" \
  "s_waitcnt lgkmcnt(0)\n\t" \
  "v_cmp_gt_u32_e32 vcc, 11, v236\n\t" \
  "s_cbranch_vccnz SPINP_%=\n\t" \
  "BACKP_%=:\n\t" \
  "ds_read_b128 v[176:179], v216 offset:16384\n\t" \
  "ds_read_b128 v[180:183], v216 offset:24576\n\t" \
  "ds_read_b128 v[184:187], v217 offset:16384\n\t" \
  "ds_read_b128 v[188:191], v217 offset:24576\n\t" \
  "s_waitcnt lgkmcnt(2)\n\t" \
  "v_mfma_f32_32x32x16_bf16 v[96:111], v[176:179], v[64:67], 0\n\t" \
  "ds_read_b128 v[176:179], v218 offset:16384\n\t" \
  "v_mfma_f32_32x32x16_bf16 v[112:127], v[180:183], v[64:67], 0\n\t" \
  "ds_read_b128 v[180:183], v218 offset:24576\n\t" \
  "s_waitcnt lgkmcnt(2)\n\t" \
  "v_mfma_f32_32x32x16_bf16 v[96:111], v[184:187], v[68:71], v[96:111]\n\t" \
  "ds_read_b128 v[184:187], v219 offset:16384\n\t" \
  "v_mfma_f32_32x32x16_bf16 v[112:127], v[188:191], v[68:71], v[112:127]\n\t" \
  "ds_read_b128 v[188:191], v219 offset:24576\n\t" \
  "s_waitcnt lgkmcnt(2)\n\t" \
  "v_mfma_f32_32x32x16_bf16 v[96:111], v[176:179], v[72:75], v[96:111]\n\t" \
  "ds_read_b128 v[176:179], v220 offset:16384\n\t" \
  "v_mfma_f32_32x32x16_bf16 v[112:127], v[180:183], v[72:75], v[112:127]\n\t" \
  "ds_read_b128 v[180:183], v220 offset:24576\n\t" \
  "s_waitcnt lgkmcnt(2)\n\t" \
  "v_mfma_f32_32x32x16_bf16 v[96:111], v[184:187], v[76:79], v[96:111]\n\t" \
  "ds_read_b128 v[184:187], v221 offset:16384\n\t" \
  "v_mfma_f32_32x32x16_bf16 v[112:127], v[188:191], v[76:79], v[112:127]\n\t" \
  "ds_read_b128 v[188:191], v221 offset:24576\n\t" \
  "s_waitcnt lgkmcnt(2)\n\t" \
  "v_mfma_f32_32x32x16_bf16 v[96:111], v[176:179], v[80:83], v[96:111]\n\t" \
  "ds_read_b128 v[176:179], v222 offset:16384\n\t" \
  "v_mfma_f32_32x32x16_bf16 v[112:127], v[180:183], v[80:83], v[112:127]\n\t" \
  "ds_read_b128 v[180:183], v222 offset:24576\n\t" \
  "s_waitcnt lgkmcnt(2)\n\t" \
  "v_mfma_f32_32x32x16_bf16 v[96:111], v[184:187], v[84:87], v[96:111]\n\t" \
  "ds_read_b128 v[184:187], v223 offset:16384\n\t" \
  "v_mfma_f32_32x32x16_bf16 v[112:127], v[188:191], v[84:87], v[112:127]\n\t" \
  "ds_read_b128 v[188:191], v223 offset:24576\n\t" \
  "s_waitcnt lgkmcnt(2)\n\t" \
  "v_mfma_f32_32x32x16_bf16 v[96:111], v[176:179], v[88:91], v[96:111]\n\t" \
  "v_mfma_f32_32x32x16_bf16 v[112:127], v[180:183], v[88:91], v[112:127]\n\t" \
  "s_waitcnt lgkmcnt(0)\n\t" \
  "v_mfma_f32_32x32x16_bf16 v[96:111], v[184:187], v[92:95], v[96:111]\n\t" \
  "v_mfma_f32_32x32x16_bf16 v[112:127], v[188:191], v[92:95], v[112:127]\n\t" \
  "v_mov_b32_e32 v237, 9\n\t" \
  "ds_write_b32 v229, v237\n\t" \
  "s_nop 8\n\t" \
  "v_max3_f32 v233, v96, v97, v98\n\t" \
  "v_max3_f32 v233, v233, v99, v100\n\t" \
  "v_max3_f32 v233, v233, v101, v102\n\t" \
  "v_max3_f32 v234, v112, v113, v114\n\t" \
  "v_max3_f32 v233, v233, v103, v104\n\t" \
  "v_max3_f32 v234, v234, v115, v116\n\t" \
  "v_max3_f32 v233, v233, v105, v106\n\t" \
  "v_max3_f32 v234, v234, v117, v118\n\t" \
  "v_max3_f32 v233, v233, v107, v108\n\t" \
  "v_max3_f32 v234, v234, v119, v120\n\t" \
  "v_max3_f32 v233, v233, v109, v110\n\t" \
  "v_max3_f32 v234, v234, v121, v122\n\t" \
  "v_max3_f32 v234, v234, v123, v124\n\t" \
  "v_max3_f32 v234, v234, v125, v126\n\t" \
  "v_max3_f32 v233, v233, v111, v127\n\t" \
  "v_max_f32_e32 v233, v233, v234\n\t" \
  "v_mov_b32_e32 v235, v233\n\t" \
  "ds_read_b32 v236, v228\n\t" \
  "s_nop 0\n\t" \
  "v_permlane32_swap_b32_e32 v233, v235\n\t" \
  "v_max_f32_e32 v233, v233, v235\n\t" \
  "v_mov_b32_e32 v230, v233\n\t" \
  "v_sub_f32_e32 v96, v96, v233\n\t" \
  "v_sub_f32_e32 v97, v97, v233\n\t" \
  "v_sub_f32_e32 v98, v98, v233\n\t" \
  "v_sub_f32_e32 v99, v99, v233\n\t" \
  "v_sub_f32_e32 v100, v100, v233\n\t" \
  "v_sub_f32_e32 v101, v101, v233\n\t" \
  "v_sub_f32_e32 v102, v102, v233\n\t" \
  "v_sub_f32_e32 v103, v103, v233\n\t" \
  "v_sub_f32_e32 v104, v104, v233\n\t" \
  "v_sub_f32_e32 v105, v105, v233\n\t" \
  "v_sub_f32_e32 v106, v106, v233\n\t" \
  "v_sub_f32_e32 v107, v107, v233\n\t" \
  "v_sub_f32_e32 v108, v108, v233\n\t" \
  "v_sub_f32_e32 v109, v109, v233\n\t" \
  "v_sub_f32_e32 v110, v110, v233\n\t" \
  "v_sub_f32_e32 v111, v111, v233\n\t" \
  "s_waitcnt lgkmcnt(0)\n\t" \
  "v_cmp_gt_u32_e32 vcc, 12, v236\n\t" \
  "s_cbranch_vccnz SPINB0_%=\n\t" \
  "BACKB0_%=:\n\t" \
  "ds_read_b128 v[176:179], v216 offset:32768\n\t" \
  "ds_read_b128 v[180:183], v216 offset:40960\n\t" \
  "ds_read_b128 v[184:187], v217 offset:32768\n\t" \
  "ds_read_b128 v[188:191], v217 offset:40960\n\t" \
  "v_exp_f32_e32 v96, v96\n\t" \
  "v_exp_f32_e32 v97, v97\n\t" \
  "v_exp_f32_e32 v98, v98\n\t" \
  "v_exp_f32_e32 v99, v99\n\t" \
  "v_exp_f32_e32 v100, v100\n\t" \
  "v_exp_f32_e32 v101, v101\n\t" \
  "v_exp_f32_e32 v102, v102\n\t" \
  "v_exp_f32_e32 v103, v103\n\t" \
  "v_exp_f32_e32 v104, v104\n\t" \
  "v_exp_f32_e32 v105, v105\n\t" \
  "v_exp_f32_e32 v106, v106\n\t" \
  "v_exp_f32_e32 v107, v107\n\t" \
  "v_exp_f32_e32 v108, v108\n\t" \
  "v_exp_f32_e32 v109, v109\n\t" \
  "v_exp_f32_e32 v110, v110\n\t" \
  "v_exp_f32_e32 v111, v111\n\t" \
  "v_sub_f32_e32 v112, v112, v233\n\t" \
  "v_sub_f32_e32 v113, v113, v233\n\t" \
  "v_sub_f32_e32 v114, v114, v233\n\t" \
  "v_sub_f32_e32 v115, v115, v233\n\t" \
  "v_sub_f32_e32 v116, v116, v233\n\t" \
  "v_sub_f32_e32 v117, v117, v233\n\t" \
  "v_sub_f32_e32 v118, v118, v233\n\t" \
  "v_sub_f32_e32 v119, v119, v233\n\t" \
  "v_sub_f32_e32 v120, v120, v233\n\t" \
  "v_sub_f32_e32 v121, v121, v233\n\t" \
  "v_sub_f32_e32 v122, v122, v233\n\t" \
  "v_sub_f32_e32 v123, v123, v233\n\t" \
  "v_sub_f32_e32 v124, v124, v233\n\t" \
  "v_sub_f32_e32 v125, v125, v233\n\t" \
  "v_sub_f32_e32 v126, v126, v233\n\t" \
  "v_sub_f32_e32 v127, v127, v233\n\t" \
  "v_sub_f32_e32 v160, 0, v233\n\t" \
  "v_sub_f32_e32 v161, 0, v233\n\t" \
  "v_sub_f32_e32 v162, 0, v233\n\t" \
  "v_sub_f32_e32 v163, 0, v233\n\t" \
  "v_sub_f32_e32 v164, 0, v233\n\t" \
  "v_sub_f32_e32 v165, 0, v233\n\t" \
  "v_sub_f32_e32 v166, 0, v233\n\t" \
  "v_sub_f32_e32 v167, 0, v233\n\t" \
  "v_sub_f32_e32 v168, 0, v233\n\t" \
  "v_sub_f32_e32 v169, 0, v233\n\t" \
  "v_sub_f32_e32 v170, 0, v233\n\t" \
  "v_sub_f32_e32 v171, 0, v233\n\t" \
  "v_sub_f32_e32 v172, 0, v233\n\t" \
  "v_sub_f32_e32 v173, 0, v233\n\t" \
  "v_sub_f32_e32 v174, 0, v233\n\t" \
  "v_sub_f32_e32 v175, 0, v233\n\t" \
  "v_mov_b32_e32 v231, 0\n\t" \
  "v_mov_b32_e32 v232, 0\n\t" \
  "v_add_f32_e32 v231, v231, v96\n\t" \
  "v_add_f32_e32 v232, v232, v97\n\t" \
  "v_add_f32_e32 v231, v231, v98\n\t" \
  "v_add_f32_e32 v232, v232, v99\n\t" \
  "s_waitcnt lgkmcnt(2)\n\t" \
  "v_mfma_f32_32x32x16_bf16 v[128:143], v[176:179], v[64:67], v[160:175]\n\t" \
  "ds_read_b128 v[176:179], v218 offset:32768\n\t" \
  "v_exp_f32_e32 v112, v112\n\t" \
  "v_exp_f32_e32 v113, v113\n\t" \
  "v_cvt_pk_bf16_f32 v96, v96, v97\n\t" \
  "v_mfma_f32_32x32x16_bf16 v[144:159], v[180:183], v[64:67], v[160:175]\n\t" \
  "ds_read_b128 v[180:183], v218 offset:40960\n\t" \
  "v_exp_f32_e32 v114, v114\n\t" \
  "v_exp_f32_e32 v115, v115\n\t" \
  "v_add_f32_e32 v231, v231, v100\n\t" \
  "s_waitcnt lgkmcnt(2)\n\t" \
  "v_mfma_f32_32x32x16_bf16 v[128:143], v[184:187], v[68:71], v[128:143]\n\t" \
  "ds_read_b128 v[184:187], v219 offset:32768\n\t" \
  "v_exp_f32_e32 v116, v116\n\t" \
  "v_exp_f32_e32 v117, v117\n\t" \
  "v_cvt_pk_bf16_f32 v97, v98, v99\n\t" \
  "v_mfma_f32_32x32x16_bf16 v[144:159], v[188:191], v[68:71], v[144:159]\n\t" \
  "ds_read_b128 v[188:191], v219 offset:40960\n\t" \
  "v_exp_f32_e32 v118, v118\n\t" \
  "v_exp_f32_e32 v119, v119\n\t" \
  "v_add_f32_e32 v232, v232, v101\n\t" \
  "s_waitcnt lgkmcnt(2)\n\t" \
  "v_mfma_f32_32x32x16_bf16 v[128:143], v[176:179], v[72:75], v[128:143]\n\t" \
  "ds_read_b128 v[176:179], v220 offset:32768\n\t" \
  "v_exp_f32_e32 v120, v120\n\t" \
  "v_exp_f32_e32 v121, v121\n\t" \
  "v_cvt_pk_bf16_f32 v98, v100, v101\n\t" \
  "v_mfma_f32_32x32x16_bf16 v[144:159], v[180:183], v[72:75], v[144:159]\n\t" \
  "ds_read_b128 v[180:183], v220 offset:40960\n\t" \
  "v_exp_f32_e32 v122, v122\n\t" \
  "v_exp_f32_e32 v123, v123\n\t" \
  "v_add_f32_e32 v231, v231, v102\n\t" \
  "s_waitcnt lgkmcnt(2)\n\t" \
  "v_mfma_f32_32x32x16_bf16 v[128:143], v[184:187], v[76:79], v[128:143]\n\t" \
  "ds_read_b128 v[184:187], v221 offset:32768\n\t" \
  "v_exp_f32_e32 v124, v124\n\t" \
  "v_exp_f32_e32 v125, v125\n\t" \
  "v_cvt_pk_bf16_f32 v99, v102, v103\n\t" \
  "v_mfma_f32_32x32x16_bf16 v[144:159], v[188:191], v[76:79], v[144:159]\n\t" \
  "ds_read_b128 v[188:191], v221 offset:40960\n\t" \
  "ds_read_b32 v236, v228\n\t" \
  "v_exp_f32_e32 v126, v126\n\t" \
  "v_exp_f32_e32 v127, v127\n\t" \
  "s_waitcnt lgkmcnt(3)\n\t" \
  "v_mfma_f32_32x32x16_bf16 v[128:143], v[176:179], v[80:83], v[128:143]\n\t" \
  "ds_read_b128 v[176:179], v222 offset:32768\n\t" \
  "v_add_f32_e32 v232, v232, v103\n\t" \
  "v_add_f32_e32 v231, v231, v104\n\t" \
  "v_add_f32_e32 v232, v232, v105\n\t" \
  "v_add_f32_e32 v231, v231, v106\n\t" \
  "v_add_f32_e32 v232, v232, v107\n\t" \
  "v_mfma_f32_32x32x16_bf16 v[144:159], v[180:183], v[80:83], v[144:159]\n\t" \
  "ds_read_b128 v[180:183], v222 offset:40960\n\t" \
  "s_waitcnt lgkmcnt(2)\n\t" \
  "v_cmp_gt_u32_e32 vcc, 13, v236\n\t" \
  "s_cbranch_vccnz SPINA1_%=\n\t" \
  "BACKA1_%=:\n\t" \
  "v_cvt_pk_bf16_f32 v104, v104, v105\n\t" \
  "v_add_f32_e32 v231, v231, v108\n\t" \
  "v_cvt_pk_bf16_f32 v105, v106, v107\n\t" \
  "v_mfma_f32_32x32x16_bf16 v[128:143], v[184:187], v[84:87], v[128:143]\n\t" \
  "ds_read_b128 v[184:187], v223 offset:32768\n\t" \
  "ds_read_b64_tr_b16 v[192:193], v224 offset:0\n\t" \
  "ds_read_b64_tr_b16 v[194:195], v224 offset:2048\n\t" \
  "v_add_f32_e32 v232, v232, v109\n\t" \
  "v_cvt_pk_bf16_f32 v106, v108, v109\n\t" \
  "v_add_f32_e32 v231, v231, v110\n\t" \
  "v_mfma_f32_32x32x16_bf16 v[144:159], v[188:191], v[84:87], v[144:159]\n\t" \
  "ds_read_b128 v[188:191], v223 offset:40960\n\t" \
  "ds_read_b64_tr_b16 v[196:197], v225 offset:0\n\t" \
  "ds_read_b64_tr_b16 v[198:199], v225 offset:2048\n\t" \
  "v_cvt_pk_bf16_f32 v107, v110, v111\n\t" \
  "v_add_f32_e32 v232, v232, v111\n\t" \
  "v_add_f32_e32 v231, v231, v112\n\t" \
  "s_waitcnt lgkmcnt(6)\n\t" \
  "v_mfma_f32_32x32x16_bf16 v[128:143], v[176:179], v[88:91], v[128:143]\n\t" \
  "ds_read_b64_tr_b16 v[200:201], v226 offset:0\n\t" \
  "ds_read_b64_tr_b16 v[202:203], v226 offset:2048\n\t" \
  "v_add_f32_e32 v232, v232, v113\n\t" \
  "v_add_f32_e32 v231, v231, v114\n\t" \
  "v_add_f32_e32 v232, v232, v115\n\t" \
  "v_cvt_pk_bf16_f32 v112, v112, v113\n\t" \
  "v_mfma_f32_32x32x16_bf16 v[144:159], v[180:183], v[88:91], v[144:159]\n\t" \
  "ds_read_b64_tr_b16 v[204:205], v227 offset:0\n\t" \
  "ds_read_b64_tr_b16 v[206:207], v227 offset:2048\n\t" \
  "v_add_f32_e32 v231, v231, v116\n\t" \
  "v_cvt_pk_bf16_f32 v113, v114, v115\n\t" \
  "v_add_f32_e32 v232, v232, v117\n\t" \
  "v_cvt_pk_bf16_f32 v114, v116, v117\n\t" \
  "s_waitcnt lgkmcnt(6)\n\t" \
  "v_mfma_f32_32x32x16_bf16 v[128:143], v[184:187], v[92:95], v[128:143]\n\t" \
  "ds_read_b64_tr_b16 v[208:209], v224 offset:4096\n\t" \
  "ds_read_b64_tr_b16 v[210:211], v224 offset:6144\n\t" \
  "v_add_f32_e32 v231, v231, v118\n\t" \
  "v_cvt_pk_bf16_f32 v115, v118, v119\n\t" \
  "v_add_f32_e32 v232, v232, v119\n\t" \
  "v_add_f32_e32 v231, v231, v120\n\t" \
  "v_mfma_f32_32x32x16_bf16 v[144:159], v[188:191], v[92:95], v[144:159]\n\t" \
  "ds_read_b64_tr_b16 v[212:213], v225 offset:4096\n\t" \
  "ds_read_b64_tr_b16 v[214:215], v225 offset:6144\n\t" \
  "v_add_f32_e32 v232, v232, v121\n\t" \
  "v_add_f32_e32 v231, v231, v122\n\t" \
  "v_add_f32_e32 v232, v232, v123\n\t" \
  "v_add_f32_e32 v231, v231, v124\n\t" \
  "v_add_f32_e32 v232, v232, v125\n\t" \
  "v_add_f32_e32 v231, v231, v126\n\t" \
  "v_add_f32_e32 v232, v232, v127\n\t" \
  "v_mov_b32_e32 v237, 10\n\t" \
  "ds_write_b32 v229, v237\n\t" \
  "s_waitcnt lgkmcnt(9)\n\t" \
  "v_mfma_f32_32x32x16_bf16 v[0:15], v[192:195], v[96:99], 0\n\t" \
  "ds_read_b64_tr_b16 v[192:193], v226 offset:4096\n\t" \
  "ds_read_b64_tr_b16 v[194:195], v226 offset:6144\n\t" \
  "v_cvt_pk_bf16_f32 v120, v120, v121\n\t" \
  "v_cvt_pk_bf16_f32 v121, v122, v123\n\t" \
  "v_cvt_pk_bf16_f32 v122, v124, v125\n\t" \
  "v_cvt_pk_bf16_f32 v123, v126, v127\n\t" \
  "v_mfma_f32_32x32x16_bf16 v[16:31], v[196:199], v[96:99], 0\n\t" \
  "ds_read_b64_tr_b16 v[196:197], v227 offset:4096\n\t" \
  "ds_read_b64_tr_b16 v[198:199], v227 offset:6144\n\t" \
  "v_max3_f32 v233, v128, v129, v130\n\t" \
  "v_max3_f32 v233, v233, v131, v132\n\t" \
  "v_max3_f32 v233, v233, v133, v134\n\t" \
  "v_max3_f32 v234, v144, v145, v146\n\t" \
  "s_waitcnt lgkmcnt(9)\n\t" \
  "v_mfma_f32_32x32x16_bf16 v[32:47], v[200:203], v[96:99], 0\n\t" \
  "ds_read_b64_tr_b16 v[200:201], v224 offset:8192\n\t" \
  "ds_read_b64_tr_b16 v[202:203], v224 offset:10240\n\t" \
  "v_max3_f32 v233, v233, v135, v136\n\t" \
  "v_max3_f32 v234, v234, v147, v148\n\t" \
  "v_max3_f32 v233, v233, v137, v138\n\t" \
  "v_max3_f32 v234, v234, v149, v150\n\t" \
  "v_mfma_f32_32x32x16_bf16 v[48:63], v[204:207], v[96:99], 0\n\t" \
  "ds_read_b64_tr_b16 v[204:205], v225 offset:8192\n\t" \
  "ds_read_b64_tr_b16 v[206:207], v225 offset:10240\n\t" \
  "v_max3_f32 v233, v233, v139, v140\n\t" \
  "v_max3_f32 v234, v234, v151, v152\n\t" \
  "v_max3_f32 v233, v233, v141, v142\n\t" \
  "v_max3_f32 v234, v234, v153, v154\n\t" \
  "s_waitcnt lgkmcnt(9)\n\t" \
  "v_mfma_f32_32x32x16_bf16 v[0:15], v[208:211], v[104:107], v[0:15]\n\t" \
  "ds_read_b64_tr_b16 v[208:209], v226 offset:8192\n\t" \
  "ds_read_b64_tr_b16 v[210:211], v226 offset:10240\n\t" \
  "v_max3_f32 v234, v234, v155, v156\n\t" \
  "v_max3_f32 v234, v234, v157, v158\n\t" \
  "v_max3_f32 v233, v233, v143, v159\n\t" \
  "v_max_f32_e32 v233, v233, v234\n\t" \
  "v_mfma_f32_32x32x16_bf16 v[16:31], v[212:215], v[104:107], v[16:31]\n\t" \
  "ds_read_b64_tr_b16 v[212:213], v227 offset:8192\n\t" \
  "ds_read_b64_tr_b16 v[214:215], v227 offset:10240\n\t" \
  "v_mov_b32_e32 v235, v233\n\t" \
  "s_waitcnt lgkmcnt(8)\n\t" \
  "v_mfma_f32_32x32x16_bf16 v[32:47], v[192:195], v[104:107], v[32:47]\n\t" \
  "ds_read_b64_tr_b16 v[192:193], v224 offset:12288\n\t" \
  "ds_read_b64_tr_b16 v[194:195], v224 offset:14336\n\t" \
  "v_permlane32_swap_b32_e32 v233, v235\n\t" \
  "v_max_f32_e32 v233, v233, v235\n\t" \
  "v_cmp_lt_f32_e32 vcc, 0x41000000, v233\n\t" \
  "s_cbranch_vccnz RARE1_%=\n\t" \
  "v_mfma_f32_32x32x16_bf16 v[48:63], v[196:199], v[104:107], v[48:63]\n\t" \
  "ds_read_b64_tr_b16 v[196:197], v225 offset:12288\n\t" \
  "ds_read_b64_tr_b16 v[198:199], v225 offset:14336\n\t" \
  "v_exp_f32_e32 v128, v128\n\t" \
  "v_exp_f32_e32 v129, v129\n\t" \
  "s_waitcnt lgkmcnt(8)\n\t" \
  "v_mfma_f32_32x32x16_bf16 v[0:15], v[200:203], v[112:115], v[0:15]\n\t" \
  "ds_read_b64_tr_b16 v[200:201], v226 offset:12288\n\t" \
  "ds_read_b64_tr_b16 v[202:203], v226 offset:14336\n\t" \
  "v_exp_f32_e32 v130, v130\n\t" \
  "v_exp_f32_e32 v131, v131\n\t" \
  "v_mfma_f32_32x32x16_bf16 v[16:31], v[204:207], v[112:115], v[16:31]\n\t" \
  "ds_read_b64_tr_b16 v[204:205], v227 offset:12288\n\t" \
  "ds_read_b64_tr_b16 v[206:207], v227 offset:14336\n\t" \
  "ds_read_b32 v236, v228\n\t" \
  "v_exp_f32_e32 v132, v132\n\t" \
  "s_waitcnt lgkmcnt(9)\n\t" \
  "v_mfma_f32_32x32x16_bf16 v[32:47], v[208:211], v[112:115], v[32:47]\n\t" \
  "v_exp_f32_e32 v133, v133\n\t" \
  "v_exp_f32_e32 v134, v134\n\t" \
  "v_exp_f32_e32 v135, v135\n\t" \
  "v_mfma_f32_32x32x16_bf16 v[48:63], v[212:215], v[112:115], v[48:63]\n\t" \
  "s_waitcnt lgkmcnt(0)\n\t" \
  "v_cmp_gt_u32_e32 vcc, 15, v236\n\t" \
  "s_cbranch_vccnz SPINB1c_%=\n\t" \
  "BACKB1c_%=:\n\t" \
  "v_exp_f32_e32 v136, v136\n\t" \
  "v_exp_f32_e32 v137, v137\n\t" \
  "v_mfma_f32_32x32x16_bf16 v[0:15], v[192:195], v[120:123], v[0:15]\n\t" \
  "ds_read_b128 v[176:179], v216 offset:49152\n\t" \
  "v_exp_f32_e32 v138, v138\n\t" \
  "v_exp_f32_e32 v139, v139\n\t" \
  "v_mfma_f32_32x32x16_bf16 v[16:31], v[196:199], v[120:123], v[16:31]\n\t" \
  "ds_read_b128 v[180:183], v216 offset:57344\n\t" \
  "v_exp_f32_e32 v140, v140\n\t" \
  "v_exp_f32_e32 v141, v141\n\t" \
  "v_mfma_f32_32x32x16_bf16 v[32:47], v[200:203], v[120:123], v[32:47]\n\t" \
  "ds_read_b128 v[184:187], v217 offset:49152\n\t" \
  "v_exp_f32_e32 v142, v142\n\t" \
  "v_exp_f32_e32 v143, v143\n\t" \
  "v_add_f32_e32 v231, v231, v128\n\t" \
  "v_mfma_f32_32x32x16_bf16 v[48:63], v[204:207], v[120:123], v[48:63]\n\t" \
  "ds_read_b128 v[188:191], v217 offset:57344\n\t" \
  "v_add_f32_e32 v232, v232, v129\n\t" \
  "v_add_f32_e32 v231, v231, v130\n\t" \
  "v_add_f32_e32 v232, v232, v131\n\t" \
  "v_mov_b32_e32 v237, 11\n\t" \
  "ds_write_b32 v229, v237\n\t" \
  "NEXT1_%=:\n\t" \
  "s_waitcnt lgkmcnt(3)\n\t" \
  "v_mfma_f32_32x32x16_bf16 v[96:111], v[176:179], v[64:67], v[160:175]\n\t" \
  "ds_read_b128 v[176:179], v218 offset:49152\n\t" \
  "v_exp_f32_e32 v144, v144\n\t" \
  "v_exp_f32_e32 v145, v145\n\t" \
  "v_cvt_pk_bf16_f32 v128, v128, v129\n\t" \
  "v_mfma_f32_32x32x16_bf16 v[112:127], v[180:183], v[64:67], v[160:175]\n\t" \
  "ds_read_b128 v[180:183], v218 offset:57344\n\t" \
  "v_exp_f32_e32 v146, v146\n\t" \
  "v_exp_f32_e32 v147, v147\n\t" \
  "v_add_f32_e32 v231, v231, v132\n\t" \
  "s_waitcnt lgkmcnt(3)\n\t" \
  "v_mfma_f32_32x32x16_bf16 v[96:111], v[184:187], v[68:71], v[96:111]\n\t" \
  "ds_read_b128 v[184:187], v219 offset:49152\n\t" \
  "v_exp_f32_e32 v148, v148\n\t" \
  "v_exp_f32_e32 v149, v149\n\t" \
  "v_cvt_pk_bf16_f32 v129, v130, v131\n\t" \
  "v_mfma_f32_32x32x16_bf16 v[112:127], v[188:191], v[68:71], v[112:127]\n\t" \
  "ds_read_b128 v[188:191], v219 offset:57344\n\t" \
  "v_exp_f32_e32 v150, v150\n\t" \
  "v_exp_f32_e32 v151, v151\n\t" \
  "v_add_f32_e32 v232, v232, v133\n\t" \
  "s_waitcnt lgkmcnt(2)\n\t" \
  "v_mfma_f32_32x32x16_bf16 v[96:111], v[176:179], v[72:75], v[96:111]\n\t" \
  "ds_read_b128 v[176:179], v220 offset:49152\n\t" \
  "v_exp_f32_e32 v152, v152\n\t" \
  "v_exp_f32_e32 v153, v153\n\t" \
  "v_cvt_pk_bf16_f32 v130, v132, v133\n\t" \
  "v_mfma_f32_32x32x16_bf16 v[112:127], v[180:183], v[72:75], v[112:127]\n\t" \
  "ds_read_b128 v[180:183], v220 offset:57344\n\t" \
  "v_exp_f32_e32 v154, v154\n\t" \
  "v_exp_f32_e32 v155, v155\n\t" \
  "v_add_f32_e32 v231, v231, v134\n\t" \
  "s_waitcnt lgkmcnt(2)\n\t" \
  "v_mfma_f32_32x32x16_bf16 v[96:111], v[184:187], v[76:79], v[96:111]\n\t" \
  "ds_read_b128 v[184:187], v221 offset:49152\n\t" \
  "v_exp_f32_e32 v156, v156\n\t" \
  "v_exp_f32_e32 v157, v157\n\t" \
  "v_cvt_pk_bf16_f32 v131, v134, v135\n\t" \
  "v_mfma_f32_32x32x16_bf16 v[112:127], v[188:191], v[76:79], v[112:127]\n\t" \
  "ds_read_b128 v[188:191], v221 offset:57344\n\t" \
  "v_exp_f32_e32 v158, v158\n\t" \
  "v_exp_f32_e32 v159, v159\n\t" \
  "v_add_f32_e32 v232, v232, v135\n\t" \
  "s_waitcnt lgkmcnt(2)\n\t" \
  "v_mfma_f32_32x32x16_bf16 v[96:111], v[176:179], v[80:83], v[96:111]\n\t" \
  "ds_read_b128 v[176:179], v222 offset:49152\n\t" \
  "v_add_f32_e32 v231, v231, v136\n\t" \
  "v_add_f32_e32 v232, v232, v137\n\t" \
  "v_add_f32_e32 v231, v231, v138\n\t" \
  "v_add_f32_e32 v232, v232, v139\n\t" \
  "v_cvt_pk_bf16_f32 v136, v136, v137\n\t" \
  "v_mfma_f32_32x32x16_bf16 v[112:127], v[180:183], v[80:83], v[112:127]\n\t" \
  "ds_read_b128 v[180:183], v222 offset:57344\n\t" \
  "v_add_f32_e32 v231, v231, v140\n\t" \
  "v_cvt_pk_bf16_f32 v137, v138, v139\n\t" \
  "v_add_f32_e32 v232, v232, v141\n\t" \
  "v_cvt_pk_bf16_f32 v138, v140, v141\n\t" \
  "v_add_f32_e32 v231, v231, v142\n\t" \
  "s_waitcnt lgkmcnt(2)\n\t" \
  "v_mfma_f32_32x32x16_bf16 v[96:111], v[184:187], v[84:87], v[96:111]\n\t" \
  "ds_read_b128 v[184:187], v223 offset:49152\n\t" \
  "ds_read_b64_tr_b16 v[192:193], v224 offset:16384\n\t" \
  "ds_read_b64_tr_b16 v[194:195], v224 offset:18432\n\t" \
  "v_cvt_pk_bf16_f32 v139, v142, v143\n\t" \
  "v_add_f32_e32 v232, v232, v143\n\t" \
  "v_add_f32_e32 v231, v231, v144\n\t" \
  "v_mfma_f32_32x32x16_bf16 v[112:127], v[188:191], v[84:87], v[112:127]\n\t" \
  "ds_read_b128 v[188:191], v223 offset:57344\n\t" \
  "ds_read_b64_tr_b16 v[196:197], v225 offset:16384\n\t" \
  "ds_read_b64_tr_b16 v[198:199], v225 offset:18432\n\t" \
  "v_add_f32_e32 v232, v232, v145\n\t" \
  "v_add_f32_e32 v231, v231, v146\n\t" \
  "v_add_f32_e32 v232, v232, v147\n\t" \
  "s_waitcnt lgkmcnt(6)\n\t" \
  "v_mfma_f32_32x32x16_bf16 v[96:111], v[176:179], v[88:91], v[96:111]\n\t" \
  "ds_read_b64_tr_b16 v[200:201], v226 offset:16384\n\t" \
  "ds_read_b64_tr_b16 v[202:203], v226 offset:18432\n\t" \
  "v_cvt_pk_bf16_f32 v144, v144, v145\n\t" \
  "v_add_f32_e32 v231, v231, v148\n\t" \
  "v_cvt_pk_bf16_f32 v145, v146, v147\n\t" \
  "v_add_f32_e32 v232, v232, v149\n\t" \
  "v_mfma_f32_32x32x16_bf16 v[112:127], v[180:183], v[88:91], v[112:127]\n\t" \
  "ds_read_b64_tr_b16 v[204:205], v227 offset:16384\n\t" \
  "ds_read_b64_tr_b16 v[206:207], v227 offset:18432\n\t" \
  "v_cvt_pk_bf16_f32 v146, v148, v149\n\t" \
  "v_add_f32_e32 v231, v231, v150\n\t" \
  "v_cvt_pk_bf16_f32 v147, v150, v151\n\t" \
  "v_add_f32_e32 v232, v232, v151\n\t" \
  "s_waitcnt lgkmcnt(6)\n\t" \
  "v_mfma_f32_32x32x16_bf16 v[96:111], v[184:187], v[92:95], v[96:111]\n\t" \
  "ds_read_b64_tr_b16 v[208:209], v224 offset:20480\n\t" \
  "ds_read_b64_tr_b16 v[210:211], v224 offset:22528\n\t" \
  "v_add_f32_e32 v231, v231, v152\n\t" \
  "v_add_f32_e32 v232, v232, v153\n\t" \
  "v_add_f32_e32 v231, v231, v154\n\t" \
  "v_add_f32_e32 v232, v232, v155\n\t" \
  "v_mfma_f32_32x32x16_bf16 v[112:127], v[188:191], v[92:95], v[112:127]\n\t" \
  "ds_read_b64_tr_b16 v[212:213], v225 offset:20480\n\t" \
  "ds_read_b64_tr_b16 v[214:215], v225 offset:22528\n\t" \
  "v_add_f32_e32 v231, v231, v156\n\t" \
  "v_add_f32_e32 v232, v232, v157\n\t" \
  "v_add_f32_e32 v231, v231, v158\n\t" \
  "v_add_f32_e32 v232, v232, v159\n\t" \
  "v_mov_b32_e32 v237, 12\n\t" \
  "ds_write_b32 v229, v237\n\t" \
  "s_waitcnt lgkmcnt(9)\n\t" \
  "v_mfma_f32_32x32x16_bf16 v[0:15], v[192:195], v[128:131], v[0:15]\n\t" \
  "ds_read_b64_tr_b16 v[192:193], v226 offset:20480\n\t" \
  "ds_read_b64_tr_b16 v[194:195], v226 offset:22528\n\t" \
  "v_cvt_pk_bf16_f32 v152, v152, v153\n\t" \
  "v_cvt_pk_bf16_f32 v153, v154, v155\n\t" \
  "v_cvt_pk_bf16_f32 v154, v156, v157\n\t" \
  "v_cvt_pk_bf16_f32 v155, v158, v159\n\t" \
  "v_mfma_f32_32x32x16_bf16 v[16:31], v[196:199], v[128:131], v[16:31]\n\t" \
  "ds_read_b64_tr_b16 v[196:197], v227 offset:20480\n\t" \
  "ds_read_b64_tr_b16 v[198:199], v227 offset:22528\n\t" \
  "v_max3_f32 v233, v96, v97, v98\n\t" \
  "v_max3_f32 v233, v233, v99, v100\n\t" \
  "v_max3_f32 v233, v233, v101, v102\n\t" \
  "v_max3_f32 v234, v112, v113, v114\n\t" \
  "s_waitcnt lgkmcnt(9)\n\t" \
  "v_mfma_f32_32x32x16_bf16 v[32:47], v[200:203], v[128:131], v[32:47]\n\t" \
  "ds_read_b64_tr_b16 v[200:201], v224 offset:24576\n\t" \
  "ds_read_b64_tr_b16 v[202:203], v224 offset:26624\n\t" \
  "v_max3_f32 v233, v233, v103, v104\n\t" \
  "v_max3_f32 v234, v234, v115, v116\n\t" \
  "v_max3_f32 v233, v233, v105, v106\n\t" \
  "v_max3_f32 v234, v234, v117, v118\n\t" \
  "v_mfma_f32_32x32x16_bf16 v[48:63], v[204:207], v[128:131], v[48:63]\n\t" \
  "ds_read_b64_tr_b16 v[204:205], v225 offset:24576\n\t" \
  "ds_read_b64_tr_b16 v[206:207], v225 offset:26624\n\t" \
  "v_max3_f32 v233, v233, v107, v108\n\t" \
  "v_max3_f32 v234, v234, v119, v120\n\t" \
  "v_max3_f32 v233, v233, v109, v110\n\t" \
  "v_max3_f32 v234, v234, v121, v122\n\t" \
  "s_waitcnt lgkmcnt(9)\n\t" \
  "v_mfma_f32_32x32x16_bf16 v[0:15], v[208:211], v[136:139], v[0:15]\n\t" \
  "ds_read_b64_tr_b16 v[208:209], v226 offset:24576\n\t" \
  "ds_read_b64_tr_b16 v[210:211], v226 offset:26624\n\t" \
  "v_max3_f32 v234, v234, v123, v124\n\t" \
  "v_max3_f32 v234, v234, v125, v126\n\t" \
  "v_max3_f32 v233, v233, v111, v127\n\t" \
  "v_max_f32_e32 v233, v233, v234\n\t" \
  "v_mfma_f32_32x32x16_bf16 v[16:31], v[212:215], v[136:139], v[16:31]\n\t" \
  "ds_read_b64_tr_b16 v[212:213], v227 offset:24576\n\t" \
  "ds_read_b64_tr_b16 v[214:215], v227 offset:26624\n\t" \
  "v_mov_b32_e32 v235, v233\n\t" \
  "s_waitcnt lgkmcnt(8)\n\t" \
  "v_mfma_f32_32x32x16_bf16 v[32:47], v[192:195], v[136:139], v[32:47]\n\t" \
  "ds_read_b64_tr_b16 v[192:193], v224 offset:28672\n\t" \
  "ds_read_b64_tr_b16 v[194:195], v224 offset:30720\n\t" \
  "v_permlane32_swap_b32_e32 v233, v235\n\t" \
  "v_max_f32_e32 v233, v233, v235\n\t" \
  "v_cmp_lt_f32_e32 vcc, 0x41000000, v233\n\t" \
  "s_cbranch_vccnz RARE2_%=\n\t" \
  "v_mfma_f32_32x32x16_bf16 v[48:63], v[196:199], v[136:139], v[48:63]\n\t" \
  "ds_read_b64_tr_b16 v[196:197], v225 offset:28672\n\t" \
  "ds_read_b64_tr_b16 v[198:199], v225 offset:30720\n\t" \
  "v_exp_f32_e32 v96, v96\n\t" \
  "v_exp_f32_e32 v97, v97\n\t" \
  "s_waitcnt lgkmcnt(8)\n\t" \
  "v_mfma_f32_32x32x16_bf16 v[0:15], v[200:203], v[144:147], v[0:15]\n\t" \
  "ds_read_b64_tr_b16 v[200:201], v226 offset:28672\n\t" \
  "ds_read_b64_tr_b16 v[202:203], v226 offset:30720\n\t" \
  "v_exp_f32_e32 v98, v98\n\t" \
  "v_exp_f32_e32 v99, v99\n\t" \
  "v_mfma_f32_32x32x16_bf16 v[16:31], v[204:207], v[144:147], v[16:31]\n\t" \
  "ds_read_b64_tr_b16 v[204:205], v227 offset:28672\n\t" \
  "ds_read_b64_tr_b16 v[206:207], v227 offset:30720\n\t" \
  "ds_read_b32 v236, v228\n\t" \
  "v_exp_f32_e32 v100, v100\n\t" \
  "s_waitcnt lgkmcnt(9)\n\t" \
  "v_mfma_f32_32x32x16_bf16 v[32:47], v[208:211], v[144:147], v[32:47]\n\t" \
  "v_exp_f32_e32 v101, v101\n\t" \
  "v_exp_f32_e32 v102, v102\n\t" \
  "v_exp_f32_e32 v103, v103\n\t" \
  "v_mfma_f32_32x32x16_bf16 v[48:63], v[212:215], v[144:147], v[48:63]\n\t" \
  "s_waitcnt lgkmcnt(0)\n\t" \
  "v_cmp_gt_u32_e32 vcc, 16, v236\n\t" \
  "s_cbranch_vccnz SPINB2c_%=\n\t" \
  "BACKB2c_%=:\n\t" \
  "v_exp_f32_e32 v104, v104\n\t" \
  "v_exp_f32_e32 v105, v105\n\t" \
  "v_mfma_f32_32x32x16_bf16 v[0:15], v[192:195], v[152:155], v[0:15]\n\t" \
  "ds_read_b128 v[176:179], v216 offset:0\n\t" \
  "v_exp_f32_e32 v106, v106\n\t" \
  "v_exp_f32_e32 v107, v107\n\t" \
  "v_mfma_f32_32x32x16_bf16 v[16:31], v[196:199], v[152:155], v[16:31]\n\t" \
  "ds_read_b128 v[180:183], v216 offset:8192\n\t" \
  "v_exp_f32_e32 v108, v108\n\t" \
  "v_exp_f32_e32 v109, v109\n\t" \
  "v_mfma_f32_32x32x16_bf16 v[32:47], v[200:203], v[152:155], v[32:47]\n\t" \
  "ds_read_b128 v[184:187], v217 offset:0\n\t" \
  "v_exp_f32_e32 v110, v110\n\t" \
  "v_exp_f32_e32 v111, v111\n\t" \
  "v_add_f32_e32 v231, v231, v96\n\t" \
  "v_mfma_f32_32x32x16_bf16 v[48:63], v[204:207], v[152:155], v[48:63]\n\t" \
  "ds_read_b128 v[188:191], v217 offset:8192\n\t" \
  "v_add_f32_e32 v232, v232, v97\n\t" \
  "v_add_f32_e32 v231, v231, v98\n\t" \
  "v_add_f32_e32 v232, v232, v99\n\t" \
  "v_mov_b32_e32 v237, 13\n\t" \
  "ds_write_b32 v229, v237\n\t" \
  "NEXT2_%=:\n\t" \
  "s_waitcnt lgkmcnt(3)\n\t" \
  "v_mfma_f32_32x32x16_bf16 v[128:143], v[176:179], v[64:67], v[160:175]\n\t" \
  "ds_read_b128 v[176:179], v218 offset:0\n\t" \
  "v_exp_f32_e32 v112, v112\n\t" \
  "v_exp_f32_e32 v113, v113\n\t" \
  "v_cvt_pk_bf16_f32 v96, v96, v97\n\t" \
  "v_mfma_f32_32x32x16_bf16 v[144:159], v[180:183], v[64:67], v[160:175]\n\t" \
  "ds_read_b128 v[180:183], v218 offset:8192\n\t" \
  "v_exp_f32_e32 v114, v114\n\t" \
  "v_exp_f32_e32 v115, v115\n\t" \
  "v_add_f32_e32 v231, v231, v100\n\t" \
  "s_waitcnt lgkmcnt(3)\n\t" \
  "v_mfma_f32_32x32x16_bf16 v[128:143], v[184:187], v[68:71], v[128:143]\n\t" \
  "ds_read_b128 v[184:187], v219 offset:0\n\t" \
  "v_exp_f32_e32 v116, v116\n\t" \
  "v_exp_f32_e32 v117, v117\n\t" \
  "v_cvt_pk_bf16_f32 v97, v98, v99\n\t" \
  "v_mfma_f32_32x32x16_bf16 v[144:159], v[188:191], v[68:71], v[144:159]\n\t" \
  "ds_read_b128 v[188:191], v219 offset:8192\n\t" \
  "v_exp_f32_e32 v118, v118\n\t" \
  "v_exp_f32_e32 v119, v119\n\t" \
  "v_add_f32_e32 v232, v232, v101\n\t" \
  "s_waitcnt lgkmcnt(2)\n\t" \
  "v_mfma_f32_32x32x16_bf16 v[128:143], v[176:179], v[72:75], v[128:143]\n\t" \
  "ds_read_b128 v[176:179], v220 offset:0\n\t" \
  "v_exp_f32_e32 v120, v120\n\t" \
  "v_exp_f32_e32 v121, v121\n\t" \
  "v_cvt_pk_bf16_f32 v98, v100, v101\n\t" \
  "v_mfma_f32_32x32x16_bf16 v[144:159], v[180:183], v[72:75], v[144:159]\n\t" \
  "ds_read_b128 v[180:183], v220 offset:8192\n\t" \
  "v_exp_f32_e32 v122, v122\n\t" \
  "v_exp_f32_e32 v123, v123\n\t" \
  "v_add_f32_e32 v231, v231, v102\n\t" \
  "s_waitcnt lgkmcnt(2)\n\t" \
  "v_mfma_f32_32x32x16_bf16 v[128:143], v[184:187], v[76:79], v[128:143]\n\t" \
  "ds_read_b128 v[184:187], v221 offset:0\n\t" \
  "v_exp_f32_e32 v124, v124\n\t" \
  "v_exp_f32_e32 v125, v125\n\t" \
  "v_cvt_pk_bf16_f32 v99, v102, v103\n\t" \
  "v_mfma_f32_32x32x16_bf16 v[144:159], v[188:191], v[76:79], v[144:159]\n\t" \
  "ds_read_b128 v[188:191], v221 offset:8192\n\t" \
  "ds_read_b32 v236, v228\n\t" \
  "v_exp_f32_e32 v126, v126\n\t" \
  "v_exp_f32_e32 v127, v127\n\t" \
  "s_waitcnt lgkmcnt(3)\n\t" \
  "v_mfma_f32_32x32x16_bf16 v[128:143], v[176:179], v[80:83], v[128:143]\n\t" \
  "ds_read_b128 v[176:179], v222 offset:0\n\t" \
  "v_add_f32_e32 v232, v232, v103\n\t" \
  "v_add_f32_e32 v231, v231, v104\n\t" \
  "v_add_f32_e32 v232, v232, v105\n\t" \
  "v_add_f32_e32 v231, v231, v106\n\t" \
  "v_add_f32_e32 v232, v232, v107\n\t" \
  "v_mfma_f32_32x32x16_bf16 v[144:159], v[180:183], v[80:83], v[144:159]\n\t" \
  "ds_read_b128 v[180:183], v222 offset:8192\n\t" \
  "s_waitcnt lgkmcnt(2)\n\t" \
  "v_cmp_gt_u32_e32 vcc, 17, v236\n\t" \
  "s_cbranch_vccnz SPINA3_%=\n\t" \
  "BACKA3_%=:\n\t" \
  "v_cvt_pk_bf16_f32 v104, v104, v105\n\t" \
  "v_add_f32_e32 v231, v231, v108\n\t" \
  "v_cvt_pk_bf16_f32 v105, v106, v107\n\t" \
  "v_mfma_f32_32x32x16_bf16 v[128:143], v[184:187], v[84:87], v[128:143]\n\t" \
  "ds_read_b128 v[184:187], v223 offset:0\n\t" \
  "ds_read_b64_tr_b16 v[192:193], v224 offset:32768\n\t" \
  "ds_read_b64_tr_b16 v[194:195], v224 offset:34816\n\t" \
  "v_add_f32_e32 v232, v232, v109\n\t" \
  "v_cvt_pk_bf16_f32 v106, v108, v109\n\t" \
  "v_add_f32_e32 v231, v231, v110\n\t" \
  "v_mfma_f32_32x32x16_bf16 v[144:159], v[188:191], v[84:87], v[144:159]\n\t" \
  "ds_read_b128 v[188:191], v223 offset:8192\n\t" \
  "ds_read_b64_tr_b16 v[196:197], v225 offset:32768\n\t" \
  "ds_read_b64_tr_b16 v[198:199], v225 offset:34816\n\t" \
  "v_cvt_pk_bf16_f32 v107, v110, v111\n\t" \
  "v_add_f32_e32 v232, v232, v111\n\t" \
  "v_add_f32_e32 v231, v231, v112\n\t" \
  "s_waitcnt lgkmcnt(6)\n\t" \
  "v_mfma_f32_32x32x16_bf16 v[128:143], v[176:179], v[88:91], v[128:143]\n\t" \
  "ds_read_b64_tr_b16 v[200:201], v226 offset:32768\n\t" \
  "ds_read_b64_tr_b16 v[202:203], v226 offset:34816\n\t" \
  "v_add_f32_e32 v232, v232, v113\n\t" \
  "v_add_f32_e32 v231, v231, v114\n\t" \
  "v_add_f32_e32 v232, v232, v115\n\t" \
  "v_cvt_pk_bf16_f32 v112, v112, v113\n\t" \
  "v_mfma_f32_32x32x16_bf16 v[144:159], v[180:183], v[88:91], v[144:159]\n\t" \
  "ds_read_b64_tr_b16 v[204:205], v227 offset:32768\n\t" \
  "ds_read_b64_tr_b16 v[206:207], v227 offset:34816\n\t" \
  "v_add_f32_e32 v231, v231, v116\n\t" \
  "v_cvt_pk_bf16_f32 v113, v114, v115\n\t" \
  "v_add_f32_e32 v232, v232, v117\n\t" \
  "v_cvt_pk_bf16_f32 v114, v116, v117\n\t" \
  "s_waitcnt lgkmcnt(6)\n\t" \
  "v_mfma_f32_32x32x16_bf16 v[128:143], v[184:187], v[92:95], v[128:143]\n\t" \
  "ds_read_b64_tr_b16 v[208:209], v224 offset:36864\n\t" \
  "ds_read_b64_tr_b16 v[210:211], v224 offset:38912\n\t" \
  "v_add_f32_e32 v231, v231, v118\n\t" \
  "v_cvt_pk_bf16_f32 v115, v118, v119\n\t" \
  "v_add_f32_e32 v232, v232, v119\n\t" \
  "v_add_f32_e32 v231, v231, v120\n\t" \
  "v_mfma_f32_32x32x16_bf16 v[144:159], v[188:191], v[92:95], v[144:159]\n\t" \
  "ds_read_b64_tr_b16 v[212:213], v225 offset:36864\n\t" \
  "ds_read_b64_tr_b16 v[214:215], v225 offset:38912\n\t" \
  "v_add_f32_e32 v232, v232, v121\n\t" \
  "v_add_f32_e32 v231, v231, v122\n\t" \
  "v_add_f32_e32 v232, v232, v123\n\t" \
  "v_add_f32_e32 v231, v231, v124\n\t" \
  "v_add_f32_e32 v232, v232, v125\n\t" \
  "v_add_f32_e32 v231, v231, v126\n\t" \
  "v_add_f32_e32 v232, v232, v127\n\t" \
  "v_mov_b32_e32 v237, 14\n\t" \
  "ds_write_b32 v229, v237\n\t" \
  "s_waitcnt lgkmcnt(9)\n\t" \
  "v_mfma_f32_32x32x16_bf16 v[0:15], v[192:195], v[96:99], v[0:15]\n\t" \
  "ds_read_b64_tr_b16 v[192:193], v226 offset:36864\n\t" \
  "ds_read_b64_tr_b16 v[194:195], v226 offset:38912\n\t" \
  "v_cvt_pk_bf16_f32 v120, v120, v121\n\t" \
  "v_cvt_pk_bf16_f32 v121, v122, v123\n\t" \
  "v_cvt_pk_bf16_f32 v122, v124, v125\n\t" \
  "v_cvt_pk_bf16_f32 v123, v126, v127\n\t" \
  "v_mfma_f32_32x32x16_bf16 v[16:31], v[196:199], v[96:99], v[16:31]\n\t" \
  "ds_read_b64_tr_b16 v[196:197], v227 offset:36864\n\t" \
  "ds_read_b64_tr_b16 v[198:199], v227 offset:38912\n\t" \
  "v_max3_f32 v233, v128, v129, v130\n\t" \
  "v_max3_f32 v233, v233, v131, v132\n\t" \
  "v_max3_f32 v233, v233, v133, v134\n\t" \
  "v_max3_f32 v234, v144, v145, v146\n\t" \
  "s_waitcnt lgkmcnt(9)\n\t" \
  "v_mfma_f32_32x32x16_bf16 v[32:47], v[200:203], v[96:99], v[32:47]\n\t" \
  "ds_read_b64_tr_b16 v[200:201], v224 offset:40960\n\t" \
  "ds_read_b64_tr_b16 v[202:203], v224 offset:43008\n\t" \
  "v_max3_f32 v233, v233, v135, v136\n\t" \
  "v_max3_f32 v234, v234, v147, v148\n\t" \
  "v_max3_f32 v233, v233, v137, v138\n\t" \
  "v_max3_f32 v234, v234, v149, v150\n\t" \
  "v_mfma_f32_32x32x16_bf16 v[48:63], v[204:207], v[96:99], v[48:63]\n\t" \
  "ds_read_b64_tr_b16 v[204:205], v225 offset:40960\n\t" \
  "ds_read_b64_tr_b16 v[206:207], v225 offset:43008\n\t" \
  "v_max3_f32 v233, v233, v139, v140\n\t" \
  "v_max3_f32 v234, v234, v151, v152\n\t" \
  "v_max3_f32 v233, v233, v141, v142\n\t" \
  "v_max3_f32 v234, v234, v153, v154\n\t" \
  "s_waitcnt lgkmcnt(9)\n\t" \
  "v_mfma_f32_32x32x16_bf16 v[0:15], v[208:211], v[104:107], v[0:15]\n\t" \
  "ds_read_b64_tr_b16 v[208:209], v226 offset:40960\n\t" \
  "ds_read_b64_tr_b16 v[210:211], v226 offset:43008\n\t" \
  "v_max3_f32 v234, v234, v155, v156\n\t" \
  "v_max3_f32 v234, v234, v157, v158\n\t" \
  "v_max3_f32 v233, v233, v143, v159\n\t" \
  "v_max_f32_e32 v233, v233, v234\n\t" \
  "v_mfma_f32_32x32x16_bf16 v[16:31], v[212:215], v[104:107], v[16:31]\n\t" \
  "ds_read_b64_tr_b16 v[212:213], v227 offset:40960\n\t" \
  "ds_read_b64_tr_b16 v[214:215], v227 offset:43008\n\t" \
  "v_mov_b32_e32 v235, v233\n\t" \
  "s_waitcnt lgkmcnt(8)\n\t" \
  "v_mfma_f32_32x32x16_bf16 v[32:47], v[192:195], v[104:107], v[32:47]\n\t" \
  "ds_read_b64_tr_b16 v[192:193], v224 offset:45056\n\t" \
  "ds_read_b64_tr_b16 v[194:195], v224 offset:47104\n\t" \
  "v_permlane32_swap_b32_e32 v233, v235\n\t" \
  "v_max_f32_e32 v233, v233, v235\n\t" \
  "v_cmp_lt_f32_e32 vcc, 0x41000000, v233\n\t" \
  "s_cbranch_vccnz RARE3_%=\n\t" \
  "v_mfma_f32_32x32x16_bf16 v[48:63], v[196:199], v[104:107], v[48:63]\n\t" \
  "ds_read_b64_tr_b16 v[196:197], v225 offset:45056\n\t" \
  "ds_read_b64_tr_b16 v[198:199], v225 offset:47104\n\t" \
  "v_exp_f32_e32 v128, v128\n\t" \
  "v_exp_f32_e32 v129, v129\n\t" \
  "s_waitcnt lgkmcnt(8)\n\t" \
  "v_mfma_f32_32x32x16_bf16 v[0:15], v[200:203], v[112:115], v[0:15]\n\t" \
  "ds_read_b64_tr_b16 v[200:201], v226 offset:45056\n\t" \
  "ds_read_b64_tr_b16 v[202:203], v226 offset:47104\n\t" \
  "v_exp_f32_e32 v130, v130\n\t" \
  "v_exp_f32_e32 v131, v131\n\t" \
  "v_mfma_f32_32x32x16_bf16 v[16:31], v[204:207], v[112:115], v[16:31]\n\t" \
  "ds_read_b64_tr_b16 v[204:205], v227 offset:45056\n\t" \
  "ds_read_b64_tr_b16 v[206:207], v227 offset:47104\n\t" \
  "ds_read_b32 v236, v228\n\t" \
  "v_exp_f32_e32 v132, v132\n\t" \
  "s_waitcnt lgkmcnt(9)\n\t" \
  "v_mfma_f32_32x32x16_bf16 v[32:47], v[208:211], v[112:115], v[32:47]\n\t" \
  "v_exp_f32_e32 v133, v133\n\t" \
  "v_exp_f32_e32 v134, v134\n\t" \
  "v_exp_f32_e32 v135, v135\n\t" \
  "v_mfma_f32_32x32x16_bf16 v[48:63], v[212:215], v[112:115], v[48:63]\n\t" \
  "s_waitcnt lgkmcnt(0)\n\t" \
  "v_cmp_gt_u32_e32 vcc, 19, v236\n\t" \
  "s_cbranch_vccnz SPINB3c_%=\n\t" \
  "BACKB3c_%=:\n\t" \
  "v_exp_f32_e32 v136, v136\n\t" \
  "v_exp_f32_e32 v137, v137\n\t" \
  "v_mfma_f32_32x32x16_bf16 v[0:15], v[192:195], v[120:123], v[0:15]\n\t" \
  "ds_read_b128 v[176:179], v216 offset:16384\n\t" \
  "v_exp_f32_e32 v138, v138\n\t" \
  "v_exp_f32_e32 v139, v139\n\t" \
  "v_mfma_f32_32x32x16_bf16 v[16:31], v[196:199], v[120:123], v[16:31]\n\t" \
  "ds_read_b128 v[180:183], v216 offset:24576\n\t" \
  "v_exp_f32_e32 v140, v140\n\t" \
  "v_exp_f32_e32 v141, v141\n\t" \
  "v_mfma_f32_32x32x16_bf16 v[32:47], v[200:203], v[120:123], v[32:47]\n\t" \
  "ds_read_b128 v[184:187], v217 offset:16384\n\t" \
  "v_exp_f32_e32 v142, v142\n\t" \
  "v_exp_f32_e32 v143, v143\n\t" \
  "v_add_f32_e32 v231, v231, v128\n\t" \
  "v_mfma_f32_32x32x16_bf16 v[48:63], v[204:207], v[120:123], v[48:63]\n\t" \
  "ds_read_b128 v[188:191], v217 offset:24576\n\t" \
  "v_add_f32_e32 v232, v232, v129\n\t" \
  "v_add_f32_e32 v231, v231, v130\n\t" \
  "v_add_f32_e32 v232, v232, v131\n\t" \
  "v_mov_b32_e32 v237, 15\n\t" \
  "ds_write_b32 v229, v237\n\t" \
  "NEXT3_%=:\n\t" \
  "s_waitcnt lgkmcnt(3)\n\t" \
  "v_mfma_f32_32x32x16_bf16 v[96:111], v[176:179], v[64:67], v[160:175]\n\t" \
  "ds_read_b128 v[176:179], v218 offset:16384\n\t" \
  "v_exp_f32_e32 v144, v144\n\t" \
  "v_exp_f32_e32 v145, v145\n\t" \
  "v_cvt_pk_bf16_f32 v128, v128, v129\n\t" \
  "v_mfma_f32_32x32x16_bf16 v[112:127], v[180:183], v[64:67], v[160:175]\n\t" \
  "ds_read_b128 v[180:183], v218 offset:24576\n\t" \
  "v_exp_f32_e32 v146, v146\n\t" \
  "v_exp_f32_e32 v147, v147\n\t" \
  "v_add_f32_e32 v231, v231, v132\n\t" \
  "s_waitcnt lgkmcnt(3)\n\t" \
  "v_mfma_f32_32x32x16_bf16 v[96:111], v[184:187], v[68:71], v[96:111]\n\t" \
  "ds_read_b128 v[184:187], v219 offset:16384\n\t" \
  "v_exp_f32_e32 v148, v148\n\t" \
  "v_exp_f32_e32 v149, v149\n\t" \
  "v_cvt_pk_bf16_f32 v129, v130, v131\n\t" \
  "v_mfma_f32_32x32x16_bf16 v[112:127], v[188:191], v[68:71], v[112:127]\n\t" \
  "ds_read_b128 v[188:191], v219 offset:24576\n\t" \
  "v_exp_f32_e32 v150, v150\n\t" \
  "v_exp_f32_e32 v151, v151\n\t" \
  "v_add_f32_e32 v232, v232, v133\n\t" \
  "s_waitcnt lgkmcnt(2)\n\t" \
  "v_mfma_f32_32x32x16_bf16 v[96:111], v[176:179], v[72:75], v[96:111]\n\t" \
  "ds_read_b128 v[176:179], v220 offset:16384\n\t" \
  "v_exp_f32_e32 v152, v152\n\t" \
  "v_exp_f32_e32 v153, v153\n\t" \
  "v_cvt_pk_bf16_f32 v130, v132, v133\n\t" \
  "v_mfma_f32_32x32x16_bf16 v[112:127], v[180:183], v[72:75], v[112:127]\n\t" \
  "ds_read_b128 v[180:183], v220 offset:24576\n\t" \
  "v_exp_f32_e32 v154, v154\n\t" \
  "v_exp_f32_e32 v155, v155\n\t" \
  "v_add_f32_e32 v231, v231, v134\n\t" \
  "s_waitcnt lgkmcnt(2)\n\t" \
  "v_mfma_f32_32x32x16_bf16 v[96:111], v[184:187], v[76:79], v[96:111]\n\t" \
  "ds_read_b128 v[184:187], v221 offset:16384\n\t" \
  "v_exp_f32_e32 v156, v156\n\t" \
  "v_exp_f32_e32 v157, v157\n\t" \
  "v_cvt_pk_bf16_f32 v131, v134, v135\n\t" \
  "v_mfma_f32_32x32x16_bf16 v[112:127], v[188:191], v[76:79], v[112:127]\n\t" \
  "ds_read_b128 v[188:191], v221 offset:24576\n\t" \
  "v_exp_f32_e32 v158, v158\n\t" \
  "v_exp_f32_e32 v159, v159\n\t" \
  "v_add_f32_e32 v232, v232, v135\n\t" \
  "s_waitcnt lgkmcnt(2)\n\t" \
  "v_mfma_f32_32x32x16_bf16 v[96:111], v[176:179], v[80:83], v[96:111]\n\t" \
  "ds_read_b128 v[176:179], v222 offset:16384\n\t" \
  "v_add_f32_e32 v231, v231, v136\n\t" \
  "v_add_f32_e32 v232, v232, v137\n\t" \
  "v_add_f32_e32 v231, v231, v138\n\t" \
  "v_add_f32_e32 v232, v232, v139\n\t" \
  "v_cvt_pk_bf16_f32 v136, v136, v137\n\t" \
  "v_mfma_f32_32x32x16_bf16 v[112:127], v[180:183], v[80:83], v[112:127]\n\t" \
  "ds_read_b128 v[180:183], v222 offset:24576\n\t" \
  "v_add_f32_e32 v231, v231, v140\n\t" \
  "v_cvt_pk_bf16_f32 v137, v138, v139\n\t" \
  "v_add_f32_e32 v232, v232, v141\n\t" \
  "v_cvt_pk_bf16_f32 v138, v140, v141\n\t" \
  "v_add_f32_e32 v231, v231, v142\n\t" \
  "s_waitcnt lgkmcnt(2)\n\t" \
  "v_mfma_f32_32x32x16_bf16 v[96:111], v[184:187], v[84:87], v[96:111]\n\t" \
  "ds_read_b128 v[184:187], v223 offset:16384\n\t" \
  "ds_read_b64_tr_b16 v[192:193], v224 offset:49152\n\t" \
  "ds_read_b64_tr_b16 v[194:195], v224 offset:51200\n\t" \
  "v_cvt_pk_bf16_f32 v139, v142, v143\n\t" \
  "v_add_f32_e32 v232, v232, v143\n\t" \
  "v_add_f32_e32 v231, v231, v144\n\t" \
  "v_mfma_f32_32x32x16_bf16 v[112:127], v[188:191], v[84:87], v[112:127]\n\t" \
  "ds_read_b128 v[188:191], v223 offset:24576\n\t" \
  "ds_read_b64_tr_b16 v[196:197], v225 offset:49152\n\t" \
  "ds_read_b64_tr_b16 v[198:199], v225 offset:51200\n\t" \
  "v_add_f32_e32 v232, v232, v145\n\t" \
  "v_add_f32_e32 v231, v231, v146\n\t" \
  "v_add_f32_e32 v232, v232, v147\n\t" \
  "s_waitcnt lgkmcnt(6)\n\t" \
  "v_mfma_f32_32x32x16_bf16 v[96:111], v[176:179], v[88:91], v[96:111]\n\t" \
  "ds_read_b64_tr_b16 v[200:201], v226 offset:49152\n\t" \
  "ds_read_b64_tr_b16 v[202:203], v226 offset:51200\n\t" \
  "v_cvt_pk_bf16_f32 v144, v144, v145\n\t" \
  "v_add_f32_e32 v231, v231, v148\n\t" \
  "v_cvt_pk_bf16_f32 v145, v146, v147\n\t" \
  "v_add_f32_e32 v232, v232, v149\n\t" \
  "v_mfma_f32_32x32x16_bf16 v[112:127], v[180:183], v[88:91], v[112:127]\n\t" \
  "ds_read_b64_tr_b16 v[204:205], v227 offset:49152\n\t" \
  "ds_read_b64_tr_b16 v[206:207], v227 offset:51200\n\t" \
  "v_cvt_pk_bf16_f32 v146, v148, v149\n\t" \
  "v_add_f32_e32 v231, v231, v150\n\t" \
  "v_cvt_pk_bf16_f32 v147, v150, v151\n\t" \
  "v_add_f32_e32 v232, v232, v151\n\t" \
  "s_waitcnt lgkmcnt(6)\n\t" \
  "v_mfma_f32_32x32x16_bf16 v[96:111], v[184:187], v[92:95], v[96:111]\n\t" \
  "ds_read_b64_tr_b16 v[208:209], v224 offset:53248\n\t" \
  "ds_read_b64_tr_b16 v[210:211], v224 offset:55296\n\t" \
  "v_add_f32_e32 v231, v231, v152\n\t" \
  "v_add_f32_e32 v232, v232, v153\n\t" \
  "v_add_f32_e32 v231, v231, v154\n\t" \
  "v_add_f32_e32 v232, v232, v155\n\t" \
  "v_mfma_f32_32x32x16_bf16 v[112:127], v[188:191], v[92:95], v[112:127]\n\t" \
  "ds_read_b64_tr_b16 v[212:213], v225 offset:53248\n\t" \
  "ds_read_b64_tr_b16 v[214:215], v225 offset:55296\n\t" \
  "v_add_f32_e32 v231, v231, v156\n\t" \
  "v_add_f32_e32 v232, v232, v157\n\t" \
  "v_add_f32_e32 v231, v231, v158\n\t" \
  "v_add_f32_e32 v232, v232, v159\n\t" \
  "v_mov_b32_e32 v237, 16\n\t" \
  "ds_write_b32 v229, v237\n\t" \
  "s_waitcnt lgkmcnt(9)\n\t" \
  "v_mfma_f32_32x32x16_bf16 v[0:15], v[192:195], v[128:131], v[0:15]\n\t" \
  "ds_read_b64_tr_b16 v[192:193], v226 offset:53248\n\t" \
  "ds_read_b64_tr_b16 v[194:195], v226 offset:55296\n\t" \
  "v_cvt_pk_bf16_f32 v152, v152, v153\n\t" \
  "v_cvt_pk_bf16_f32 v153, v154, v155\n\t" \
  "v_cvt_pk_bf16_f32 v154, v156, v157\n\t" \
  "v_cvt_pk_bf16_f32 v155, v158, v159\n\t" \
  "v_mfma_f32_32x32x16_bf16 v[16:31], v[196:199], v[128:131], v[16:31]\n\t" \
  "ds_read_b64_tr_b16 v[196:197], v227 offset:53248\n\t" \
  "ds_read_b64_tr_b16 v[198:199], v227 offset:55296\n\t" \
  "v_max3_f32 v233, v96, v97, v98\n\t" \
  "v_max3_f32 v233, v233, v99, v100\n\t" \
  "v_max3_f32 v233, v233, v101, v102\n\t" \
  "v_max3_f32 v234, v112, v113, v114\n\t" \
  "s_waitcnt lgkmcnt(9)\n\t" \
  "v_mfma_f32_32x32x16_bf16 v[32:47], v[200:203], v[128:131], v[32:47]\n\t" \
  "ds_read_b64_tr_b16 v[200:201], v224 offset:57344\n\t" \
  "ds_read_b64_tr_b16 v[202:203], v224 offset:59392\n\t" \
  "v_max3_f32 v233, v233, v103, v104\n\t" \
  "v_max3_f32 v234, v234, v115, v116\n\t" \
  "v_max3_f32 v233, v233, v105, v106\n\t" \
  "v_max3_f32 v234, v234, v117, v118\n\t" \
  "v_mfma_f32_32x32x16_bf16 v[48:63], v[204:207], v[128:131], v[48:63]\n\t" \
  "ds_read_b64_tr_b16 v[204:205], v225 offset:57344\n\t" \
  "ds_read_b64_tr_b16 v[206:207], v225 offset:59392\n\t" \
  "v_max3_f32 v233, v233, v107, v108\n\t" \
  "v_max3_f32 v234, v234, v119, v120\n\t" \
  "v_max3_f32 v233, v233, v109, v110\n\t" \
  "v_max3_f32 v234, v234, v121, v122\n\t" \
  "s_waitcnt lgkmcnt(9)\n\t" \
  "v_mfma_f32_32x32x16_bf16 v[0:15], v[208:211], v[136:139], v[0:15]\n\t" \
  "ds_read_b64_tr_b16 v[208:209], v226 offset:57344\n\t" \
  "ds_read_b64_tr_b16 v[210:211], v226 offset:59392\n\t" \
  "v_max3_f32 v234, v234, v123, v124\n\t" \
  "v_max3_f32 v234, v234, v125, v126\n\t" \
  "v_max3_f32 v233, v233, v111, v127\n\t" \
  "v_max_f32_e32 v233, v233, v234\n\t" \
  "v_mfma_f32_32x32x16_bf16 v[16:31], v[212:215], v[136:139], v[16:31]\n\t" \
  "ds_read_b64_tr_b16 v[212:213], v227 offset:57344\n\t" \
  "ds_read_b64_tr_b16 v[214:215], v227 offset:59392\n\t" \
  "v_mov_b32_e32 v235, v233\n\t" \
  "s_waitcnt lgkmcnt(8)\n\t" \
  "v_mfma_f32_32x32x16_bf16 v[32:47], v[192:195], v[136:139], v[32:47]\n\t" \
  "ds_read_b64_tr_b16 v[192:193], v224 offset:61440\n\t" \
  "ds_read_b64_tr_b16 v[194:195], v224 offset:63488\n\t" \
  "v_permlane32_swap_b32_e32 v233, v235\n\t" \
  "v_max_f32_e32 v233, v233, v235\n\t" \
  "v_cmp_lt_f32_e32 vcc, 0x41000000, v233\n\t" \
  "s_cbranch_vccnz RARE4_%=\n\t" \
  "v_mfma_f32_32x32x16_bf16 v[48:63], v[196:199], v[136:139], v[48:63]\n\t" \
  "ds_read_b64_tr_b16 v[196:197], v225 offset:61440\n\t" \
  "ds_read_b64_tr_b16 v[198:199], v225 offset:63488\n\t" \
  "v_exp_f32_e32 v96, v96\n\t" \
  "v_exp_f32_e32 v97, v97\n\t" \
  "s_waitcnt lgkmcnt(8)\n\t" \
  "v_mfma_f32_32x32x16_bf16 v[0:15], v[200:203], v[144:147], v[0:15]\n\t" \
  "ds_read_b64_tr_b16 v[200:201], v226 offset:61440\n\t" \
  "ds_read_b64_tr_b16 v[202:203], v226 offset:63488\n\t" \
  "v_exp_f32_e32 v98, v98\n\t" \
  "v_exp_f32_e32 v99, v99\n\t" \
  "v_mfma_f32_32x32x16_bf16 v[16:31], v[204:207], v[144:147], v[16:31]\n\t" \
  "ds_read_b64_tr_b16 v[204:205], v227 offset:61440\n\t" \
  "ds_read_b64_tr_b16 v[206:207], v227 offset:63488\n\t" \
  "ds_read_b32 v236, v228\n\t" \
  "v_exp_f32_e32 v100, v100\n\t" \
  "s_waitcnt lgkmcnt(9)\n\t" \
  "v_mfma_f32_32x32x16_bf16 v[32:47], v[208:211], v[144:147], v[32:47]\n\t" \
  "v_exp_f32_e32 v101, v101\n\t" \
  "v_exp_f32_e32 v102, v102\n\t" \
  "v_exp_f32_e32 v103, v103\n\t" \
  "v_mfma_f32_32x32x16_bf16 v[48:63], v[212:215], v[144:147], v[48:63]\n\t" \
  "s_waitcnt lgkmcnt(0)\n\t" \
  "v_cmp_gt_u32_e32 vcc, 20, v236\n\t" \
  "s_cbranch_vccnz SPINB4c_%=\n\t" \
  "BACKB4c_%=:\n\t" \
  "v_exp_f32_e32 v104, v104\n\t" \
  "v_exp_f32_e32 v105, v105\n\t" \
  "v_mfma_f32_32x32x16_bf16 v[0:15], v[192:195], v[152:155], v[0:15]\n\t" \
  "ds_read_b128 v[176:179], v216 offset:32768\n\t" \
  "v_exp_f32_e32 v106, v106\n\t" \
  "v_exp_f32_e32 v107, v107\n\t" \
  "v_mfma_f32_32x32x16_bf16 v[16:31], v[196:199], v[152:155], v[16:31]\n\t" \
  "ds_read_b128 v[180:183], v216 offset:40960\n\t" \
  "v_exp_f32_e32 v108, v108\n\t" \
  "v_exp_f32_e32 v109, v109\n\t" \
  "v_mfma_f32_32x32x16_bf16 v[32:47], v[200:203], v[152:155], v[32:47]\n\t" \
  "ds_read_b128 v[184:187], v217 offset:32768\n\t" \
  "v_exp_f32_e32 v110, v110\n\t" \
  "v_exp_f32_e32 v111, v111\n\t" \
  "v_add_f32_e32 v231, v231, v96\n\t" \
  "v_mfma_f32_32x32x16_bf16 v[48:63], v[204:207], v[152:155], v[48:63]\n\t" \
  "ds_read_b128 v[188:191], v217 offset:40960\n\t" \
  "v_add_f32_e32 v232, v232, v97\n\t" \
  "v_add_f32_e32 v231, v231, v98\n\t" \
  "v_add_f32_e32 v232, v232, v99\n\t" \
  "v_mov_b32_e32 v237, 17\n\t" \
  "ds_write_b32 v229, v237\n\t" \
  "NEXT4_%=:\n\t" \
  "s_waitcnt lgkmcnt(3)\n\t" \
  "v_mfma_f32_32x32x16_bf16 v[128:143], v[176:179], v[64:67], v[160:175]\n\t" \
  "ds_read_b128 v[176:179], v218 offset:32768\n\t" \
  "v_exp_f32_e32 v112, v112\n\t" \
  "v_exp_f32_e32 v113, v113\n\t" \
  "v_cvt_pk_bf16_f32 v96, v96, v97\n\t" \
  "v_mfma_f32_32x32x16_bf16 v[144:159], v[180:183], v[64:67], v[160:175]\n\t" \
  "ds_read_b128 v[180:183], v218 offset:40960\n\t" \
  "v_exp_f32_e32 v114, v114\n\t" \
  "v_exp_f32_e32 v115, v115\n\t" \
  "v_add_f32_e32 v231, v231, v100\n\t" \
  "s_waitcnt lgkmcnt(3)\n\t" \
  "v_mfma_f32_32x32x16_bf16 v[128:143], v[184:187], v[68:71], v[128:143]\n\t" \
  "ds_read_b128 v[184:187], v219 offset:32768\n\t" \
  "v_exp_f32_e32 v116, v116\n\t" \
  "v_exp_f32_e32 v117, v117\n\t" \
  "v_cvt_pk_bf16_f32 v97, v98, v99\n\t" \
  "v_mfma_f32_32x32x16_bf16 v[144:159], v[188:191], v[68:71], v[144:159]\n\t" \
  "ds_read_b128 v[188:191], v219 offset:40960\n\t" \
  "v_exp_f32_e32 v118, v118\n\t" \
  "v_exp_f32_e32 v119, v119\n\t" \
  "v_add_f32_e32 v232, v232, v101\n\t" \
  "s_waitcnt lgkmcnt(2)\n\t" \
  "v_mfma_f32_32x32x16_bf16 v[128:143], v[176:179], v[72:75], v[128:143]\n\t" \
  "ds_read_b128 v[176:179], v220 offset:32768\n\t" \
  "v_exp_f32_e32 v120, v120\n\t" \
  "v_exp_f32_e32 v121, v121\n\t" \
  "v_cvt_pk_bf16_f32 v98, v100, v101\n\t" \
  "v_mfma_f32_32x32x16_bf16 v[144:159], v[180:183], v[72:75], v[144:159]\n\t" \
  "ds_read_b128 v[180:183], v220 offset:40960\n\t" \
  "v_exp_f32_e32 v122, v122\n\t" \
  "v_exp_f32_e32 v123, v123\n\t" \
  "v_add_f32_e32 v231, v231, v102\n\t" \
  "s_waitcnt lgkmcnt(2)\n\t" \
  "v_mfma_f32_32x32x16_bf16 v[128:143], v[184:187], v[76:79], v[128:143]\n\t" \
  "ds_read_b128 v[184:187], v221 offset:32768\n\t" \
  "v_exp_f32_e32 v124, v124\n\t" \
  "v_exp_f32_e32 v125, v125\n\t" \
  "v_cvt_pk_bf16_f32 v99, v102, v103\n\t" \
  "v_mfma_f32_32x32x16_bf16 v[144:159], v[188:191], v[76:79], v[144:159]\n\t" \
  "ds_read_b128 v[188:191], v221 offset:40960\n\t" \
  "ds_read_b32 v236, v228\n\t" \
  "v_exp_f32_e32 v126, v126\n\t" \
  "v_exp_f32_e32 v127, v127\n\t" \
  "s_waitcnt lgkmcnt(3)\n\t" \
  "v_mfma_f32_32x32x16_bf16 v[128:143], v[176:179], v[80:83], v[128:143]\n\t" \
  "ds_read_b128 v[176:179], v222 offset:32768\n\t" \
  "v_add_f32_e32 v232, v232, v103\n\t" \
  "v_add_f32_e32 v231, v231, v104\n\t" \
  "v_add_f32_e32 v232, v232, v105\n\t" \
  "v_add_f32_e32 v231, v231, v106\n\t" \
  "v_add_f32_e32 v232, v232, v107\n\t" \
  "v_mfma_f32_32x32x16_bf16 v[144:159], v[180:183], v[80:83], v[144:159]\n\t" \
  "ds_read_b128 v[180:183], v222 offset:40960\n\t" \
  "s_waitcnt lgkmcnt(2)\n\t" \
  "v_cmp_gt_u32_e32 vcc, 21, v236\n\t" \
  "s_cbranch_vccnz SPINA5_%=\n\t" \
  "BACKA5_%=:\n\t" \
  "v_cvt_pk_bf16_f32 v104, v104, v105\n\t" \
  "v_add_f32_e32 v231, v231, v108\n\t" \
  "v_cvt_pk_bf16_f32 v105, v106, v107\n\t" \
  "v_mfma_f32_32x32x16_bf16 v[128:143], v[184:187], v[84:87], v[128:143]\n\t" \
  "ds_read_b128 v[184:187], v223 offset:32768\n\t" \
  "ds_read_b64_tr_b16 v[192:193], v244 offset:0\n\t" \
  "ds_read_b64_tr_b16 v[194:195], v244 offset:2048\n\t" \
  "v_add_f32_e32 v232, v232, v109\n\t" \
  "v_cvt_pk_bf16_f32 v106, v108, v109\n\t" \
  "v_add_f32_e32 v231, v231, v110\n\t" \
  "v_mfma_f32_32x32x16_bf16 v[144:159], v[188:191], v[84:87], v[144:159]\n\t" \
  "ds_read_b128 v[188:191], v223 offset:40960\n\t" \
  "ds_read_b64_tr_b16 v[196:197], v245 offset:0\n\t" \
  "ds_read_b64_tr_b16 v[198:199], v245 offset:2048\n\t" \
  "v_cvt_pk_bf16_f32 v107, v110, v111\n\t" \
  "v_add_f32_e32 v232, v232, v111\n\t" \
  "v_add_f32_e32 v231, v231, v112\n\t" \
  "s_waitcnt lgkmcnt(6)\n\t" \
  "v_mfma_f32_32x32x16_bf16 v[128:143], v[176:179], v[88:91], v[128:143]\n\t" \
  "ds_read_b64_tr_b16 v[200:201], v246 offset:0\n\t" \
  "ds_read_b64_tr_b16 v[202:203], v246 offset:2048\n\t" \
  "v_add_f32_e32 v232, v232, v113\n\t" \
  "v_add_f32_e32 v231, v231, v114\n\t" \
  "v_add_f32_e32 v232, v232, v115\n\t" \
  "v_cvt_pk_bf16_f32 v112, v112, v113\n\t" \
  "v_mfma_f32_32x32x16_bf16 v[144:159], v[180:183], v[88:91], v[144:159]\n\t" \
  "ds_read_b64_tr_b16 v[204:205], v247 offset:0\n\t" \
  "ds_read_b64_tr_b16 v[206:207], v247 offset:2048\n\t" \
  "v_add_f32_e32 v231, v231, v116\n\t" \
  "v_cvt_pk_bf16_f32 v113, v114, v115\n\t" \
  "v_add_f32_e32 v232, v232, v117\n\t" \
  "v_cvt_pk_bf16_f32 v114, v116, v117\n\t" \
  "s_waitcnt lgkmcnt(6)\n\t" \
  "v_mfma_f32_32x32x16_bf16 v[128:143], v[184:187], v[92:95], v[128:143]\n\t" \
  "ds_read_b64_tr_b16 v[208:209], v244 offset:4096\n\t" \
  "ds_read_b64_tr_b16 v[210:211], v244 offset:6144\n\t" \
  "v_add_f32_e32 v231, v231, v118\n\t" \
  "v_cvt_pk_bf16_f32 v115, v118, v119\n\t" \
  "v_add_f32_e32 v232, v232, v119\n\t" \
  "v_add_f32_e32 v231, v231, v120\n\t" \
  "v_mfma_f32_32x32x16_bf16 v[144:159], v[188:191], v[92:95], v[144:159]\n\t" \
  "ds_read_b64_tr_b16 v[212:213], v245 offset:4096\n\t" \
  "ds_read_b64_tr_b16 v[214:215], v245 offset:6144\n\t" \
  "v_add_f32_e32 v232, v232, v121\n\t" \
  "v_add_f32_e32 v231, v231, v122\n\t" \
  "v_add_f32_e32 v232, v232, v123\n\t" \
  "v_add_f32_e32 v231, v231, v124\n\t" \
  "v_add_f32_e32 v232, v232, v125\n\t" \
  "v_add_f32_e32 v231, v231, v126\n\t" \
  "v_add_f32_e32 v232, v232, v127\n\t" \
  "v_mov_b32_e32 v237, 18\n\t" \
  "ds_write_b32 v229, v237\n\t" \
  "s_waitcnt lgkmcnt(9)\n\t" \
  "v_mfma_f32_32x32x16_bf16 v[0:15], v[192:195], v[96:99], v[0:15]\n\t" \
  "ds_read_b64_tr_b16 v[192:193], v246 offset:4096\n\t" \
  "ds_read_b64_tr_b16 v[194:195], v246 offset:6144\n\t" \
  "v_cvt_pk_bf16_f32 v120, v120, v121\n\t" \
  "v_cvt_pk_bf16_f32 v121, v122, v123\n\t" \
  "v_cvt_pk_bf16_f32 v122, v124, v125\n\t" \
  "v_cvt_pk_bf16_f32 v123, v126, v127\n\t" \
  "v_mfma_f32_32x32x16_bf16 v[16:31], v[196:199], v[96:99], v[16:31]\n\t" \
  "ds_read_b64_tr_b16 v[196:197], v247 offset:4096\n\t" \
  "ds_read_b64_tr_b16 v[198:199], v247 offset:6144\n\t" \
  "v_max3_f32 v233, v128, v129, v130\n\t" \
  "v_max3_f32 v233, v233, v131, v132\n\t" \
  "v_max3_f32 v233, v233, v133, v134\n\t" \
  "v_max3_f32 v234, v144, v145, v146\n\t" \
  "s_waitcnt lgkmcnt(9)\n\t" \
  "v_mfma_f32_32x32x16_bf16 v[32:47], v[200:203], v[96:99], v[32:47]\n\t" \
  "ds_read_b64_tr_b16 v[200:201], v244 offset:8192\n\t" \
  "ds_read_b64_tr_b16 v[202:203], v244 offset:10240\n\t" \
  "v_max3_f32 v233, v233, v135, v136\n\t" \
  "v_max3_f32 v234, v234, v147, v148\n\t" \
  "v_max3_f32 v233, v233, v137, v138\n\t" \
  "v_max3_f32 v234, v234, v149, v150\n\t" \
  "v_mfma_f32_32x32x16_bf16 v[48:63], v[204:207], v[96:99], v[48:63]\n\t" \
  "ds_read_b64_tr_b16 v[204:205], v245 offset:8192\n\t" \
  "ds_read_b64_tr_b16 v[206:207], v245 offset:10240\n\t" \
  "v_max3_f32 v233, v233, v139, v140\n\t" \
  "v_max3_f32 v234, v234, v151, v152\n\t" \
  "v_max3_f32 v233, v233, v141, v142\n\t" \
  "v_max3_f32 v234, v234, v153, v154\n\t" \
  "s_waitcnt lgkmcnt(9)\n\t" \
  "v_mfma_f32_32x32x16_bf16 v[0:15], v[208:211], v[104:107], v[0:15]\n\t" \
  "ds_read_b64_tr_b16 v[208:209], v246 offset:8192\n\t" \
  "ds_read_b64_tr_b16 v[210:211], v246 offset:10240\n\t" \
  "v_max3_f32 v234, v234, v155, v156\n\t" \
  "v_max3_f32 v234, v234, v157, v158\n\t" \
  "v_max3_f32 v233, v233, v143, v159\n\t" \
  "v_max_f32_e32 v233, v233, v234\n\t" \
  "v_mfma_f32_32x32x16_bf16 v[16:31], v[212:215], v[104:107], v[16:31]\n\t" \
  "ds_read_b64_tr_b16 v[212:213], v247 offset:8192\n\t" \
  "ds_read_b64_tr_b16 v[214:215], v247 offset:10240\n\t" \
  "v_mov_b32_e32 v235, v233\n\t" \
  "s_waitcnt lgkmcnt(8)\n\t" \
  "v_mfma_f32_32x32x16_bf16 v[32:47], v[192:195], v[104:107], v[32:47]\n\t" \
  "ds_read_b64_tr_b16 v[192:193], v244 offset:12288\n\t" \
  "ds_read_b64_tr_b16 v[194:195], v244 offset:14336\n\t" \
  "v_permlane32_swap_b32_e32 v233, v235\n\t" \
  "v_max_f32_e32 v233, v233, v235\n\t" \
  "v_cmp_lt_f32_e32 vcc, 0x41000000, v233\n\t" \
  "s_cbranch_vccnz RARE5_%=\n\t" \
  "v_mfma_f32_32x32x16_bf16 v[48:63], v[196:199], v[104:107], v[48:63]\n\t" \
  "ds_read_b64_tr_b16 v[196:197], v245 offset:12288\n\t" \
  "ds_read_b64_tr_b16 v[198:199], v245 offset:14336\n\t" \
  "v_exp_f32_e32 v128, v128\n\t" \
  "v_exp_f32_e32 v129, v129\n\t" \
  "s_waitcnt lgkmcnt(8)\n\t" \
  "v_mfma_f32_32x32x16_bf16 v[0:15], v[200:203], v[112:115], v[0:15]\n\t" \
  "ds_read_b64_tr_b16 v[200:201], v246 offset:12288\n\t" \
  "ds_read_b64_tr_b16 v[202:203], v246 offset:14336\n\t" \
  "v_exp_f32_e32 v130, v130\n\t" \
  "v_exp_f32_e32 v131, v131\n\t" \
  "v_mfma_f32_32x32x16_bf16 v[16:31], v[204:207], v[112:115], v[16:31]\n\t" \
  "ds_read_b64_tr_b16 v[204:205], v247 offset:12288\n\t" \
  "ds_read_b64_tr_b16 v[206:207], v247 offset:14336\n\t" \
  "ds_read_b32 v236, v228\n\t" \
  "v_exp_f32_e32 v132, v132\n\t" \
  "s_waitcnt lgkmcnt(9)\n\t" \
  "v_mfma_f32_32x32x16_bf16 v[32:47], v[208:211], v[112:115], v[32:47]\n\t" \
  "v_exp_f32_e32 v133, v133\n\t" \
  "v_exp_f32_e32 v134, v134\n\t" \
  "v_exp_f32_e32 v135, v135\n\t" \
  "v_mfma_f32_32x32x16_bf16 v[48:63], v[212:215], v[112:115], v[48:63]\n\t" \
  "s_waitcnt lgkmcnt(0)\n\t" \
  "v_cmp_gt_u32_e32 vcc, 23, v236\n\t" \
  "s_cbranch_vccnz SPINB5c_%=\n\t" \
  "BACKB5c_%=:\n\t" \
  "v_exp_f32_e32 v136, v136\n\t" \
  "v_exp_f32_e32 v137, v137\n\t" \
  "v_mfma_f32_32x32x16_bf16 v[0:15], v[192:195], v[120:123], v[0:15]\n\t" \
  "ds_read_b128 v[176:179], v216 offset:49152\n\t" \
  "v_exp_f32_e32 v138, v138\n\t" \
  "v_exp_f32_e32 v139, v139\n\t" \
  "v_mfma_f32_32x32x16_bf16 v[16:31], v[196:199], v[120:123], v[16:31]\n\t" \
  "ds_read_b128 v[180:183], v216 offset:57344\n\t" \
  "v_exp_f32_e32 v140, v140\n\t" \
  "v_exp_f32_e32 v141, v141\n\t" \
  "v_mfma_f32_32x32x16_bf16 v[32:47], v[200:203], v[120:123], v[32:47]\n\t" \
  "ds_read_b128 v[184:187], v217 offset:49152\n\t" \
  "v_exp_f32_e32 v142, v142\n\t" \
  "v_exp_f32_e32 v143, v143\n\t" \
  "v_add_f32_e32 v231, v231, v128\n\t" \
  "v_mfma_f32_32x32x16_bf16 v[48:63], v[204:207], v[120:123], v[48:63]\n\t" \
  "ds_read_b128 v[188:191], v217 offset:57344\n\t" \
  "v_add_f32_e32 v232, v232, v129\n\t" \
  "v_add_f32_e32 v231, v231, v130\n\t" \
  "v_add_f32_e32 v232, v232, v131\n\t" \
  "v_mov_b32_e32 v237, 19\n\t" \
  "ds_write_b32 v229, v237\n\t" \
  "NEXT5_%=:\n\t" \
  "s_waitcnt lgkmcnt(3)\n\t" \
  "v_mfma_f32_32x32x16_bf16 v[96:111], v[176:179], v[64:67], v[160:175]\n\t" \
  "ds_read_b128 v[176:179], v218 offset:49152\n\t" \
  "v_exp_f32_e32 v144, v144\n\t" \
  "v_exp_f32_e32 v145, v145\n\t" \
  "v_cvt_pk_bf16_f32 v128, v128, v129\n\t" \
  "v_mfma_f32_32x32x16_bf16 v[112:127], v[180:183], v[64:67], v[160:175]\n\t" \
  "ds_read_b128 v[180:183], v218 offset:57344\n\t" \
  "v_exp_f32_e32 v146, v146\n\t" \
  "v_exp_f32_e32 v147, v147\n\t" \
  "v_add_f32_e32 v231, v231, v132\n\t" \
  "s_waitcnt lgkmcnt(3)\n\t" \
  "v_mfma_f32_32x32x16_bf16 v[96:111], v[184:187], v[68:71], v[96:111]\n\t" \
  "ds_read_b128 v[184:187], v219 offset:49152\n\t" \
  "v_exp_f32_e32 v148, v148\n\t" \
  "v_exp_f32_e32 v149, v149\n\t" \
  "v_cvt_pk_bf16_f32 v129, v130, v131\n\t" \
  "v_mfma_f32_32x32x16_bf16 v[112:127], v[188:191], v[68:71], v[112:127]\n\t" \
  "ds_read_b128 v[188:191], v219 offset:57344\n\t" \
  "v_exp_f32_e32 v150, v150\n\t" \
  "v_exp_f32_e32 v151, v151\n\t" \
  "v_add_f32_e32 v232, v232, v133\n\t" \
  "s_waitcnt lgkmcnt(2)\n\t" \
  "v_mfma_f32_32x32x16_bf16 v[96:111], v[176:179], v[72:75], v[96:111]\n\t" \
  "ds_read_b128 v[176:179], v220 offset:49152\n\t" \
  "v_exp_f32_e32 v152, v152\n\t" \
  "v_exp_f32_e32 v153, v153\n\t" \
  "v_cvt_pk_bf16_f32 v130, v132, v133\n\t" \
  "v_mfma_f32_32x32x16_bf16 v[112:127], v[180:183], v[72:75], v[112:127]\n\t" \
  "ds_read_b128 v[180:183], v220 offset:57344\n\t" \
  "v_exp_f32_e32 v154, v154\n\t" \
  "v_exp_f32_e32 v155, v155\n\t" \
  "v_add_f32_e32 v231, v231, v134\n\t" \
  "s_waitcnt lgkmcnt(2)\n\t" \
  "v_mfma_f32_32x32x16_bf16 v[96:111], v[184:187], v[76:79], v[96:111]\n\t" \
  "ds_read_b128 v[184:187], v221 offset:49152\n\t" \
  "v_exp_f32_e32 v156, v156\n\t" \
  "v_exp_f32_e32 v157, v157\n\t" \
  "v_cvt_pk_bf16_f32 v131, v134, v135\n\t" \
  "v_mfma_f32_32x32x16_bf16 v[112:127], v[188:191], v[76:79], v[112:127]\n\t" \
  "ds_read_b128 v[188:191], v221 offset:57344\n\t" \
  "v_exp_f32_e32 v158, v158\n\t" \
  "v_exp_f32_e32 v159, v159\n\t" \
  "v_add_f32_e32 v232, v232, v135\n\t" \
  "s_waitcnt lgkmcnt(2)\n\t" \
  "v_mfma_f32_32x32x16_bf16 v[96:111], v[176:179], v[80:83], v[96:111]\n\t" \
  "ds_read_b128 v[176:179], v222 offset:49152\n\t" \
  "v_add_f32_e32 v231, v231, v136\n\t" \
  "v_add_f32_e32 v232, v232, v137\n\t" \
  "v_add_f32_e32 v231, v231, v138\n\t" \
  "v_add_f32_e32 v232, v232, v139\n\t" \
  "v_cvt_pk_bf16_f32 v136, v136, v137\n\t" \
  "v_mfma_f32_32x32x16_bf16 v[112:127], v[180:183], v[80:83], v[112:127]\n\t" \
  "ds_read_b128 v[180:183], v222 offset:57344\n\t" \
  "v_add_f32_e32 v231, v231, v140\n\t" \
  "v_cvt_pk_bf16_f32 v137, v138, v139\n\t" \
  "v_add_f32_e32 v232, v232, v141\n\t" \
  "v_cvt_pk_bf16_f32 v138, v140, v141\n\t" \
  "v_add_f32_e32 v231, v231, v142\n\t" \
  "s_waitcnt lgkmcnt(2)\n\t" \
  "v_mfma_f32_32x32x16_bf16 v[96:111], v[184:187], v[84:87], v[96:111]\n\t" \
  "ds_read_b128 v[184:187], v223 offset:49152\n\t" \
  "ds_read_b64_tr_b16 v[192:193], v224 offset:0\n\t" \
  "ds_read_b64_tr_b16 v[194:195], v224 offset:2048\n\t" \
  "v_cvt_pk_bf16_f32 v139, v142, v143\n\t" \
  "v_add_f32_e32 v232, v232, v143\n\t" \
  "v_add_f32_e32 v231, v231, v144\n\t" \
  "v_mfma_f32_32x32x16_bf16 v[112:127], v[188:191], v[84:87], v[112:127]\n\t" \
  "ds_read_b128 v[188:191], v223 offset:57344\n\t" \
  "ds_read_b64_tr_b16 v[196:197], v225 offset:0\n\t" \
  "ds_read_b64_tr_b16 v[198:199], v225 offset:2048\n\t" \
  "v_add_f32_e32 v232, v232, v145\n\t" \
  "v_add_f32_e32 v231, v231, v146\n\t" \
  "v_add_f32_e32 v232, v232, v147\n\t" \
  "s_waitcnt lgkmcnt(6)\n\t" \
  "v_mfma_f32_32x32x16_bf16 v[96:111], v[176:179], v[88:91], v[96:111]\n\t" \
  "ds_read_b64_tr_b16 v[200:201], v226 offset:0\n\t" \
  "ds_read_b64_tr_b16 v[202:203], v226 offset:2048\n\t" \
  "v_cvt_pk_bf16_f32 v144, v144, v145\n\t" \
  "v_add_f32_e32 v231, v231, v148\n\t" \
  "v_cvt_pk_bf16_f32 v145, v146, v147\n\t" \
  "v_add_f32_e32 v232, v232, v149\n\t" \
  "v_mfma_f32_32x32x16_bf16 v[112:127], v[180:183], v[88:91], v[112:127]\n\t" \
  "ds_read_b64_tr_b16 v[204:205], v227 offset:0\n\t" \
  "ds_read_b64_tr_b16 v[206:207], v227 offset:2048\n\t" \
  "v_cvt_pk_bf16_f32 v146, v148, v149\n\t" \
  "v_add_f32_e32 v231, v231, v150\n\t" \
  "v_cvt_pk_bf16_f32 v147, v150, v151\n\t" \
  "v_add_f32_e32 v232, v232, v151\n\t" \
  "s_waitcnt lgkmcnt(6)\n\t" \
  "v_mfma_f32_32x32x16_bf16 v[96:111], v[184:187], v[92:95], v[96:111]\n\t" \
  "ds_read_b64_tr_b16 v[208:209], v224 offset:4096\n\t" \
  "ds_read_b64_tr_b16 v[210:211], v224 offset:6144\n\t" \
  "v_add_f32_e32 v231, v231, v152\n\t" \
  "v_add_f32_e32 v232, v232, v153\n\t" \
  "v_add_f32_e32 v231, v231, v154\n\t" \
  "v_add_f32_e32 v232, v232, v155\n\t" \
  "v_mfma_f32_32x32x16_bf16 v[112:127], v[188:191], v[92:95], v[112:127]\n\t" \
  "ds_read_b64_tr_b16 v[212:213], v225 offset:4096\n\t" \
  "ds_read_b64_tr_b16 v[214:215], v225 offset:6144\n\t" \
  "v_add_f32_e32 v231, v231, v156\n\t" \
  "v_add_f32_e32 v232, v232, v157\n\t" \
  "v_add_f32_e32 v231, v231, v158\n\t" \
  "v_add_f32_e32 v232, v232, v159\n\t" \
  "v_mov_b32_e32 v237, 20\n\t" \
  "ds_write_b32 v229, v237\n\t" \
  "s_waitcnt lgkmcnt(9)\n\t" \
  "v_mfma_f32_32x32x16_bf16 v[0:15], v[192:195], v[128:131], v[0:15]\n\t" \
  "ds_read_b64_tr_b16 v[192:193], v226 offset:4096\n\t" \
  "ds_read_b64_tr_b16 v[194:195], v226 offset:6144\n\t" \
  "v_cvt_pk_bf16_f32 v152, v152, v153\n\t" \
  "v_cvt_pk_bf16_f32 v153, v154, v155\n\t" \
  "v_cvt_pk_bf16_f32 v154, v156, v157\n\t" \
  "v_cvt_pk_bf16_f32 v155, v158, v159\n\t" \
  "v_mfma_f32_32x32x16_bf16 v[16:31], v[196:199], v[128:131], v[16:31]\n\t" \
  "ds_read_b64_tr_b16 v[196:197], v227 offset:4096\n\t" \
  "ds_read_b64_tr_b16 v[198:199], v227 offset:6144\n\t" \
  "v_max3_f32 v233, v96, v97, v98\n\t" \
  "v_max3_f32 v233, v233, v99, v100\n\t" \
  "v_max3_f32 v233, v233, v101, v102\n\t" \
  "v_max3_f32 v234, v112, v113, v114\n\t" \
  "s_waitcnt lgkmcnt(9)\n\t" \
  "v_mfma_f32_32x32x16_bf16 v[32:47], v[200:203], v[128:131], v[32:47]\n\t" \
  "ds_read_b64_tr_b16 v[200:201], v224 offset:8192\n\t" \
  "ds_read_b64_tr_b16 v[202:203], v224 offset:10240\n\t" \
  "v_max3_f32 v233, v233, v103, v104\n\t" \
  "v_max3_f32 v234, v234, v115, v116\n\t" \
  "v_max3_f32 v233, v233, v105, v106\n\t" \
  "v_max3_f32 v234, v234, v117, v118\n\t" \
  "v_mfma_f32_32x32x16_bf16 v[48:63], v[204:207], v[128:131], v[48:63]\n\t" \
  "ds_read_b64_tr_b16 v[204:205], v225 offset:8192\n\t" \
  "ds_read_b64_tr_b16 v[206:207], v225 offset:10240\n\t" \
  "v_max3_f32 v233, v233, v107, v108\n\t" \
  "v_max3_f32 v234, v234, v119, v120\n\t" \
  "v_max3_f32 v233, v233, v109, v110\n\t" \
  "v_max3_f32 v234, v234, v121, v122\n\t" \
  "s_waitcnt lgkmcnt(9)\n\t" \
  "v_mfma_f32_32x32x16_bf16 v[0:15], v[208:211], v[136:139], v[0:15]\n\t" \
  "ds_read_b64_tr_b16 v[208:209], v226 offset:8192\n\t" \
  "ds_read_b64_tr_b16 v[210:211], v226 offset:10240\n\t" \
  "v_max3_f32 v234, v234, v123, v124\n\t" \
  "v_max3_f32 v234, v234, v125, v126\n\t" \
  "v_max3_f32 v233, v233, v111, v127\n\t" \
  "v_max_f32_e32 v233, v233, v234\n\t" \
  "v_mfma_f32_32x32x16_bf16 v[16:31], v[212:215], v[136:139], v[16:31]\n\t" \
  "ds_read_b64_tr_b16 v[212:213], v227 offset:8192\n\t" \
  "ds_read_b64_tr_b16 v[214:215], v227 offset:10240\n\t" \
  "v_mov_b32_e32 v235, v233\n\t" \
  "s_waitcnt lgkmcnt(8)\n\t" \
  "v_mfma_f32_32x32x16_bf16 v[32:47], v[192:195], v[136:139], v[32:47]\n\t" \
  "ds_read_b64_tr_b16 v[192:193], v224 offset:12288\n\t" \
  "ds_read_b64_tr_b16 v[194:195], v224 offset:14336\n\t" \
  "v_permlane32_swap_b32_e32 v233, v235\n\t" \
  "v_max_f32_e32 v233, v233, v235\n\t" \
  "v_cmp_lt_f32_e32 vcc, 0x41000000, v233\n\t" \
  "s_cbranch_vccnz RARE6_%=\n\t" \
  "v_mfma_f32_32x32x16_bf16 v[48:63], v[196:199], v[136:139], v[48:63]\n\t" \
  "ds_read_b64_tr_b16 v[196:197], v225 offset:12288\n\t" \
  "ds_read_b64_tr_b16 v[198:199], v225 offset:14336\n\t" \
  "v_exp_f32_e32 v96, v96\n\t" \
  "v_exp_f32_e32 v97, v97\n\t" \
  "s_waitcnt lgkmcnt(8)\n\t" \
  "v_mfma_f32_32x32x16_bf16 v[0:15], v[200:203], v[144:147], v[0:15]\n\t" \
  "ds_read_b64_tr_b16 v[200:201], v226 offset:12288\n\t" \
  "ds_read_b64_tr_b16 v[202:203], v226 offset:14336\n\t" \
  "v_exp_f32_e32 v98, v98\n\t" \
  "v_exp_f32_e32 v99, v99\n\t" \
  "v_mfma_f32_32x32x16_bf16 v[16:31], v[204:207], v[144:147], v[16:31]\n\t" \
  "ds_read_b64_tr_b16 v[204:205], v227 offset:12288\n\t" \
  "ds_read_b64_tr_b16 v[206:207], v227 offset:14336\n\t" \
  "ds_read_b32 v236, v228\n\t" \
  "v_exp_f32_e32 v100, v100\n\t" \
  "s_waitcnt lgkmcnt(9)\n\t" \
  "v_mfma_f32_32x32x16_bf16 v[32:47], v[208:211], v[144:147], v[32:47]\n\t" \
  "v_exp_f32_e32 v101, v101\n\t" \
  "v_exp_f32_e32 v102, v102\n\t" \
  "v_exp_f32_e32 v103, v103\n\t" \
  "v_mfma_f32_32x32x16_bf16 v[48:63], v[212:215], v[144:147], v[48:63]\n\t" \
  "s_waitcnt lgkmcnt(0)\n\t" \
  "v_cmp_gt_u32_e32 vcc, 24, v236\n\t" \
  "s_cbranch_vccnz SPINB6c_%=\n\t" \
  "BACKB6c_%=:\n\t" \
  "v_exp_f32_e32 v104, v104\n\t" \
  "v_exp_f32_e32 v105, v105\n\t" \
  "v_mfma_f32_32x32x16_bf16 v[0:15], v[192:195], v[152:155], v[0:15]\n\t" \
  "ds_read_b128 v[176:179], v216 offset:0\n\t" \
  "v_exp_f32_e32 v106, v106\n\t" \
  "v_exp_f32_e32 v107, v107\n\t" \
  "v_mfma_f32_32x32x16_bf16 v[16:31], v[196:199], v[152:155], v[16:31]\n\t" \
  "ds_read_b128 v[180:183], v216 offset:8192\n\t" \
  "v_exp_f32_e32 v108, v108\n\t" \
  "v_exp_f32_e32 v109, v109\n\t" \
  "v_mfma_f32_32x32x16_bf16 v[32:47], v[200:203], v[152:155], v[32:47]\n\t" \
  "ds_read_b128 v[184:187], v217 offset:0\n\t" \
  "v_exp_f32_e32 v110, v110\n\t" \
  "v_exp_f32_e32 v111, v111\n\t" \
  "v_add_f32_e32 v231, v231, v96\n\t" \
  "v_mfma_f32_32x32x16_bf16 v[48:63], v[204:207], v[152:155], v[48:63]\n\t" \
  "ds_read_b128 v[188:191], v217 offset:8192\n\t" \
  "v_add_f32_e32 v232, v232, v97\n\t" \
  "v_add_f32_e32 v231, v231, v98\n\t" \
  "v_add_f32_e32 v232, v232, v99\n\t" \
  "v_mov_b32_e32 v237, 21\n\t" \
  "ds_write_b32 v229, v237\n\t" \
  "NEXT6_%=:\n\t" \
  "s_waitcnt lgkmcnt(3)\n\t" \
  "v_mfma_f32_32x32x16_bf16 v[128:143], v[176:179], v[64:67], v[160:175]\n\t" \
  "ds_read_b128 v[176:179], v218 offset:0\n\t" \
  "v_exp_f32_e32 v112, v112\n\t" \
  "v_exp_f32_e32 v113, v113\n\t" \
  "v_cvt_pk_bf16_f32 v96, v96, v97\n\t" \
  "v_mfma_f32_32x32x16_bf16 v[144:159], v[180:183], v[64:67], v[160:175]\n\t" \
  "ds_read_b128 v[180:183], v218 offset:8192\n\t" \
  "v_exp_f32_e32 v114, v114\n\t" \
  "v_exp_f32_e32 v115, v115\n\t" \
  "v_add_f32_e32 v231, v231, v100\n\t" \
  "s_waitcnt lgkmcnt(3)\n\t" \
  "v_mfma_f32_32x32x16_bf16 v[128:143], v[184:187], v[68:71], v[128:143]\n\t" \
  "ds_read_b128 v[184:187], v219 offset:0\n\t" \
  "v_exp_f32_e32 v116, v116\n\t" \
  "v_exp_f32_e32 v117, v117\n\t" \
  "v_cvt_pk_bf16_f32 v97, v98, v99\n\t" \
  "v_mfma_f32_32x32x16_bf16 v[144:159], v[188:191], v[68:71], v[144:159]\n\t" \
  "ds_read_b128 v[188:191], v219 offset:8192\n\t" \
  "v_exp_f32_e32 v118, v118\n\t" \
  "v_exp_f32_e32 v119, v119\n\t" \
  "v_add_f32_e32 v232, v232, v101\n\t" \
  "s_waitcnt lgkmcnt(2)\n\t" \
  "v_mfma_f32_32x32x16_bf16 v[128:143], v[176:179], v[72:75], v[128:143]\n\t" \
  "ds_read_b128 v[176:179], v220 offset:0\n\t" \
  "v_exp_f32_e32 v120, v120\n\t" \
  "v_exp_f32_e32 v121, v121\n\t" \
  "v_cvt_pk_bf16_f32 v98, v100, v101\n\t" \
  "v_mfma_f32_32x32x16_bf16 v[144:159], v[180:183], v[72:75], v[144:159]\n\t" \
  "ds_read_b128 v[180:183], v220 offset:8192\n\t" \
  "v_exp_f32_e32 v122, v122\n\t" \
  "v_exp_f32_e32 v123, v123\n\t" \
  "v_add_f32_e32 v231, v231, v102\n\t" \
  "s_waitcnt lgkmcnt(2)\n\t" \
  "v_mfma_f32_32x32x16_bf16 v[128:143], v[184:187], v[76:79], v[128:143]\n\t" \
  "ds_read_b128 v[184:187], v221 offset:0\n\t" \
  "v_exp_f32_e32 v124, v124\n\t" \
  "v_exp_f32_e32 v125, v125\n\t" \
  "v_cvt_pk_bf16_f32 v99, v102, v103\n\t" \
  "v_mfma_f32_32x32x16_bf16 v[144:159], v[188:191], v[76:79], v[144:159]\n\t" \
  "ds_read_b128 v[188:191], v221 offset:8192\n\t" \
  "ds_read_b32 v236, v228\n\t" \
  "v_exp_f32_e32 v126, v126\n\t" \
  "v_exp_f32_e32 v127, v127\n\t" \
  "s_waitcnt lgkmcnt(3)\n\t" \
  "v_mfma_f32_32x32x16_bf16 v[128:143], v[176:179], v[80:83], v[128:143]\n\t" \
  "ds_read_b128 v[176:179], v222 offset:0\n\t" \
  "v_add_f32_e32 v232, v232, v103\n\t" \
  "v_add_f32_e32 v231, v231, v104\n\t" \
  "v_add_f32_e32 v232, v232, v105\n\t" \
  "v_add_f32_e32 v231, v231, v106\n\t" \
  "v_add_f32_e32 v232, v232, v107\n\t" \
  "v_mfma_f32_32x32x16_bf16 v[144:159], v[180:183], v[80:83], v[144:159]\n\t" \
  "ds_read_b128 v[180:183], v222 offset:8192\n\t" \
  "s_waitcnt lgkmcnt(2)\n\t" \
  "v_cmp_gt_u32_e32 vcc, 25, v236\n\t" \
  "s_cbranch_vccnz SPINA7_%=\n\t" \
  "BACKA7_%=:\n\t" \
  "v_cvt_pk_bf16_f32 v104, v104, v105\n\t" \
  "v_add_f32_e32 v231, v231, v108\n\t" \
  "v_cvt_pk_bf16_f32 v105, v106, v107\n\t" \
  "v_mfma_f32_32x32x16_bf16 v[128:143], v[184:187], v[84:87], v[128:143]\n\t" \
  "ds_read_b128 v[184:187], v223 offset:0\n\t" \
  "ds_read_b64_tr_b16 v[192:193], v224 offset:16384\n\t" \
  "ds_read_b64_tr_b16 v[194:195], v224 offset:18432\n\t" \
  "v_add_f32_e32 v232, v232, v109\n\t" \
  "v_cvt_pk_bf16_f32 v106, v108, v109\n\t" \
  "v_add_f32_e32 v231, v231, v110\n\t" \
  "v_mfma_f32_32x32x16_bf16 v[144:159], v[188:191], v[84:87], v[144:159]\n\t" \
  "ds_read_b128 v[188:191], v223 offset:8192\n\t" \
  "ds_read_b64_tr_b16 v[196:197], v225 offset:16384\n\t" \
  "ds_read_b64_tr_b16 v[198:199], v225 offset:18432\n\t" \
  "v_cvt_pk_bf16_f32 v107, v110, v111\n\t" \
  "v_add_f32_e32 v232, v232, v111\n\t" \
  "v_add_f32_e32 v231, v231, v112\n\t" \
  "s_waitcnt lgkmcnt(6)\n\t" \
  "v_mfma_f32_32x32x16_bf16 v[128:143], v[176:179], v[88:91], v[128:143]\n\t" \
  "ds_read_b64_tr_b16 v[200:201], v226 offset:16384\n\t" \
  "ds_read_b64_tr_b16 v[202:203], v226 offset:18432\n\t" \
  "v_add_f32_e32 v232, v232, v113\n\t" \
  "v_add_f32_e32 v231, v231, v114\n\t" \
  "v_add_f32_e32 v232, v232, v115\n\t" \
  "v_cvt_pk_bf16_f32 v112, v112, v113\n\t" \
  "v_mfma_f32_32x32x16_bf16 v[144:159], v[180:183], v[88:91], v[144:159]\n\t" \
  "ds_read_b64_tr_b16 v[204:205], v227 offset:16384\n\t" \
  "ds_read_b64_tr_b16 v[206:207], v227 offset:18432\n\t" \
  "v_add_f32_e32 v231, v231, v116\n\t" \
  "v_cvt_pk_bf16_f32 v113, v114, v115\n\t" \
  "v_add_f32_e32 v232, v232, v117\n\t" \
  "v_cvt_pk_bf16_f32 v114, v116, v117\n\t" \
  "s_waitcnt lgkmcnt(6)\n\t" \
  "v_mfma_f32_32x32x16_bf16 v[128:143], v[184:187], v[92:95], v[128:143]\n\t" \
  "ds_read_b64_tr_b16 v[208:209], v224 offset:20480\n\t" \
  "ds_read_b64_tr_b16 v[210:211], v224 offset:22528\n\t" \
  "v_add_f32_e32 v231, v231, v118\n\t" \
  "v_cvt_pk_bf16_f32 v115, v118, v119\n\t" \
  "v_add_f32_e32 v232, v232, v119\n\t" \
  "v_add_f32_e32 v231, v231, v120\n\t" \
  "v_mfma_f32_32x32x16_bf16 v[144:159], v[188:191], v[92:95], v[144:159]\n\t" \
  "ds_read_b64_tr_b16 v[212:213], v225 offset:20480\n\t" \
  "ds_read_b64_tr_b16 v[214:215], v225 offset:22528\n\t" \
  "v_add_f32_e32 v232, v232, v121\n\t" \
  "v_add_f32_e32 v231, v231, v122\n\t" \
  "v_add_f32_e32 v232, v232, v123\n\t" \
  "v_add_f32_e32 v231, v231, v124\n\t" \
  "v_add_f32_e32 v232, v232, v125\n\t" \
  "v_add_f32_e32 v231, v231, v126\n\t" \
  "v_add_f32_e32 v232, v232, v127\n\t" \
  "v_mov_b32_e32 v237, 22\n\t" \
  "ds_write_b32 v229, v237\n\t" \
  "s_waitcnt lgkmcnt(9)\n\t" \
  "v_mfma_f32_32x32x16_bf16 v[0:15], v[192:195], v[96:99], v[0:15]\n\t" \
  "ds_read_b64_tr_b16 v[192:193], v226 offset:20480\n\t" \
  "ds_read_b64_tr_b16 v[194:195], v226 offset:22528\n\t" \
  "v_cvt_pk_bf16_f32 v120, v120, v121\n\t" \
  "v_cvt_pk_bf16_f32 v121, v122, v123\n\t" \
  "v_cvt_pk_bf16_f32 v122, v124, v125\n\t" \
  "v_cvt_pk_bf16_f32 v123, v126, v127\n\t" \
  "v_mfma_f32_32x32x16_bf16 v[16:31], v[196:199], v[96:99], v[16:31]\n\t" \
  "ds_read_b64_tr_b16 v[196:197], v227 offset:20480\n\t" \
  "ds_read_b64_tr_b16 v[198:199], v227 offset:22528\n\t" \
  "v_max3_f32 v233, v128, v129, v130\n\t" \
  "v_max3_f32 v233, v233, v131, v132\n\t" \
  "v_max3_f32 v233, v233, v133, v134\n\t" \
  "v_max3_f32 v234, v144, v145, v146\n\t" \
  "s_waitcnt lgkmcnt(9)\n\t" \
  "v_mfma_f32_32x32x16_bf16 v[32:47], v[200:203], v[96:99], v[32:47]\n\t" \
  "ds_read_b64_tr_b16 v[200:201], v224 offset:24576\n\t" \
  "ds_read_b64_tr_b16 v[202:203], v224 offset:26624\n\t" \
  "v_max3_f32 v233, v233, v135, v136\n\t" \
  "v_max3_f32 v234, v234, v147, v148\n\t" \
  "v_max3_f32 v233, v233, v137, v138\n\t" \
  "v_max3_f32 v234, v234, v149, v150\n\t" \
  "v_mfma_f32_32x32x16_bf16 v[48:63], v[204:207], v[96:99], v[48:63]\n\t" \
  "ds_read_b64_tr_b16 v[204:205], v225 offset:24576\n\t" \
  "ds_read_b64_tr_b16 v[206:207], v225 offset:26624\n\t" \
  "v_max3_f32 v233, v233, v139, v140\n\t" \
  "v_max3_f32 v234, v234, v151, v152\n\t" \
  "v_max3_f32 v233, v233, v141, v142\n\t" \
  "v_max3_f32 v234, v234, v153, v154\n\t" \
  "s_waitcnt lgkmcnt(9)\n\t" \
  "v_mfma_f32_32x32x16_bf16 v[0:15], v[208:211], v[104:107], v[0:15]\n\t" \
  "ds_read_b64_tr_b16 v[208:209], v226 offset:24576\n\t" \
  "ds_read_b64_tr_b16 v[210:211], v226 offset:26624\n\t" \
  "v_max3_f32 v234, v234, v155, v156\n\t" \
  "v_max3_f32 v234, v234, v157, v158\n\t" \
  "v_max3_f32 v233, v233, v143, v159\n\t" \
  "v_max_f32_e32 v233, v233, v234\n\t" \
  "v_mfma_f32_32x32x16_bf16 v[16:31], v[212:215], v[104:107], v[16:31]\n\t" \
  "ds_read_b64_tr_b16 v[212:213], v227 offset:24576\n\t" \
  "ds_read_b64_tr_b16 v[214:215], v227 offset:26624\n\t" \
  "v_mov_b32_e32 v235, v233\n\t" \
  "s_waitcnt lgkmcnt(8)\n\t" \
  "v_mfma_f32_32x32x16_bf16 v[32:47], v[192:195], v[104:107], v[32:47]\n\t" \
  "ds_read_b64_tr_b16 v[192:193], v224 offset:28672\n\t" \
  "ds_read_b64_tr_b16 v[194:195], v224 offset:30720\n\t" \
  "v_permlane32_swap_b32_e32 v233, v235\n\t" \
  "v_max_f32_e32 v233, v233, v235\n\t" \
  "v_cmp_lt_f32_e32 vcc, 0x41000000, v233\n\t" \
  "s_cbranch_vccnz RARE7_%=\n\t" \
  "v_mfma_f32_32x32x16_bf16 v[48:63], v[196:199], v[104:107], v[48:63]\n\t" \
  "ds_read_b64_tr_b16 v[196:197], v225 offset:28672\n\t" \
  "ds_read_b64_tr_b16 v[198:199], v225 offset:30720\n\t" \
  "v_exp_f32_e32 v128, v128\n\t" \
  "v_exp_f32_e32 v129, v129\n\t" \
  "s_waitcnt lgkmcnt(8)\n\t" \
  "v_mfma_f32_32x32x16_bf16 v[0:15], v[200:203], v[112:115], v[0:15]\n\t" \
  "ds_read_b64_tr_b16 v[200:201], v226 offset:28672\n\t" \
  "ds_read_b64_tr_b16 v[202:203], v226 offset:30720\n\t" \
  "v_exp_f32_e32 v130, v130\n\t" \
  "v_exp_f32_e32 v131, v131\n\t" \
  "v_mfma_f32_32x32x16_bf16 v[16:31], v[204:207], v[112:115], v[16:31]\n\t" \
  "ds_read_b64_tr_b16 v[204:205], v227 offset:28672\n\t" \
  "ds_read_b64_tr_b16 v[206:207], v227 offset:30720\n\t" \
  "v_exp_f32_e32 v132, v132\n\t" \
  "v_exp_f32_e32 v133, v133\n\t" \
  "s_waitcnt lgkmcnt(8)\n\t" \
  "v_mfma_f32_32x32x16_bf16 v[32:47], v[208:211], v[112:115], v[32:47]\n\t" \
  "v_exp_f32_e32 v134, v134\n\t" \
  "v_exp_f32_e32 v135, v135\n\t" \
  "v_exp_f32_e32 v136, v136\n\t" \
  "v_mfma_f32_32x32x16_bf16 v[48:63], v[212:215], v[112:115], v[48:63]\n\t" \
  "v_exp_f32_e32 v137, v137\n\t" \
  "v_exp_f32_e32 v138, v138\n\t" \
  "v_exp_f32_e32 v139, v139\n\t" \
  "s_waitcnt lgkmcnt(4)\n\t" \
  "v_mfma_f32_32x32x16_bf16 v[0:15], v[192:195], v[120:123], v[0:15]\n\t" \
  "v_exp_f32_e32 v140, v140\n\t" \
  "v_exp_f32_e32 v141, v141\n\t" \
  "v_exp_f32_e32 v142, v142\n\t" \
  "v_mfma_f32_32x32x16_bf16 v[16:31], v[196:199], v[120:123], v[16:31]\n\t" \
  "v_exp_f32_e32 v143, v143\n\t" \
  "s_waitcnt lgkmcnt(0)\n\t" \
  "v_mfma_f32_32x32x16_bf16 v[32:47], v[200:203], v[120:123], v[32:47]\n\t" \
  "v_add_f32_e32 v231, v231, v128\n\t" \
  "v_add_f32_e32 v232, v232, v129\n\t" \
  "v_add_f32_e32 v231, v231, v130\n\t" \
  "v_add_f32_e32 v232, v232, v131\n\t" \
  "v_mfma_f32_32x32x16_bf16 v[48:63], v[204:207], v[120:123], v[48:63]\n\t" \
  "v_mov_b32_e32 v237, 23\n\t" \
  "ds_write_b32 v229, v237\n\t" \
  "NEXT7_%=:\n\t" \
  "ds_read_b32 v236, v228\n\t" \
  "s_waitcnt lgkmcnt(0)\n\t" \
  "v_cmp_gt_u32_e32 vcc, 26, v236\n\t" \
  "s_cbranch_vccnz SPIND_%=\n\t" \
  "BACKD_%=:\n\t" \
  "s_cmp_lg_u32 %[own], 0\n\t" \
  "s_cbranch_scc1 OWN_%=\n\t" \
  "ds_read_b64_tr_b16 v[192:193], v224 offset:32768\n\t" \
  "ds_read_b64_tr_b16 v[194:195], v224 offset:34816\n\t" \
  "ds_read_b64_tr_b16 v[196:197], v225 offset:32768\n\t" \
  "ds_read_b64_tr_b16 v[198:199], v225 offset:34816\n\t" \
  "ds_read_b64_tr_b16 v[200:201], v226 offset:32768\n\t" \
  "ds_read_b64_tr_b16 v[202:203], v226 offset:34816\n\t" \
  "ds_read_b64_tr_b16 v[204:205], v227 offset:32768\n\t" \
  "ds_read_b64_tr_b16 v[206:207], v227 offset:34816\n\t" \
  "ds_read_b64_tr_b16 v[208:209], v224 offset:36864\n\t" \
  "ds_read_b64_tr_b16 v[210:211], v224 offset:38912\n\t" \
  "ds_read_b64_tr_b16 v[212:213], v225 offset:36864\n\t" \
  "ds_read_b64_tr_b16 v[214:215], v225 offset:38912\n\t" \
  "v_cvt_pk_bf16_f32 v128, v128, v129\n\t" \
  "v_add_f32_e32 v231, v231, v132\n\t" \
  "v_cvt_pk_bf16_f32 v129, v130, v131\n\t" \
  "v_add_f32_e32 v232, v232, v133\n\t" \
  "v_cvt_pk_bf16_f32 v130, v132, v133\n\t" \
  "v_add_f32_e32 v231, v231, v134\n\t" \
  "v_cvt_pk_bf16_f32 v131, v134, v135\n\t" \
  "v_add_f32_e32 v232, v232, v135\n\t" \
  "s_waitcnt lgkmcnt(8)\n\t" \
  "v_mfma_f32_32x32x16_bf16 v[0:15], v[192:195], v[128:131], v[0:15]\n\t" \
  "ds_read_b64_tr_b16 v[192:193], v226 offset:36864\n\t" \
  "ds_read_b64_tr_b16 v[194:195], v226 offset:38912\n\t" \
  "v_add_f32_e32 v231, v231, v136\n\t" \
  "v_add_f32_e32 v232, v232, v137\n\t" \
  "v_add_f32_e32 v231, v231, v138\n\t" \
  "v_add_f32_e32 v232, v232, v139\n\t" \
  "v_mfma_f32_32x32x16_bf16 v[16:31], v[196:199], v[128:131], v[16:31]\n\t" \
  "ds_read_b64_tr_b16 v[196:197], v227 offset:36864\n\t" \
  "ds_read_b64_tr_b16 v[198:199], v227 offset:38912\n\t" \
  "v_cvt_pk_bf16_f32 v136, v136, v137\n\t" \
  "v_add_f32_e32 v231, v231, v140\n\t" \
  "v_cvt_pk_bf16_f32 v137, v138, v139\n\t" \
  "v_add_f32_e32 v232, v232, v141\n\t" \
  "s_waitcnt lgkmcnt(8)\n\t" \
  "v_mfma_f32_32x32x16_bf16 v[32:47], v[200:203], v[128:131], v[32:47]\n\t" \
  "ds_read_b64_tr_b16 v[200:201], v224 offset:40960\n\t" \
  "ds_read_b64_tr_b16 v[202:203], v224 offset:43008\n\t" \
  "v_cvt_pk_bf16_f32 v138, v140, v141\n\t" \
  "v_add_f32_e32 v231, v231, v142\n\t" \
  "v_cvt_pk_bf16_f32 v139, v142, v143\n\t" \
  "v_mfma_f32_32x32x16_bf16 v[48:63], v[204:207], v[128:131], v[48:63]\n\t" \
  "ds_read_b64_tr_b16 v[204:205], v224 offset:45056\n\t" \
  "ds_read_b64_tr_b16 v[206:207], v224 offset:47104\n\t" \
  "v_exp_f32_e32 v144, v144\n\t" \
  "v_exp_f32_e32 v145, v145\n\t" \
  "s_waitcnt lgkmcnt(8)\n\t" \
  "v_mfma_f32_32x32x16_bf16 v[0:15], v[208:211], v[136:139], v[0:15]\n\t" \
  "ds_read_b64_tr_b16 v[208:209], v225 offset:40960\n\t" \
  "ds_read_b64_tr_b16 v[210:211], v225 offset:43008\n\t" \
  "v_exp_f32_e32 v146, v146\n\t" \
  "v_exp_f32_e32 v147, v147\n\t" \
  "v_mfma_f32_32x32x16_bf16 v[16:31], v[212:215], v[136:139], v[16:31]\n\t" \
  "ds_read_b64_tr_b16 v[212:213], v225 offset:45056\n\t" \
  "ds_read_b64_tr_b16 v[214:215], v225 offset:47104\n\t" \
  "v_exp_f32_e32 v148, v148\n\t" \
  "v_exp_f32_e32 v149, v149\n\t" \
  "s_waitcnt lgkmcnt(8)\n\t" \
  "v_mfma_f32_32x32x16_bf16 v[32:47], v[192:195], v[136:139], v[32:47]\n\t" \
  "ds_read_b64_tr_b16 v[192:193], v226 offset:40960\n\t" \
  "ds_read_b64_tr_b16 v[194:195], v226 offset:43008\n\t" \
  "v_exp_f32_e32 v150, v150\n\t" \
  "v_exp_f32_e32 v151, v151\n\t" \
  "v_mfma_f32_32x32x16_bf16 v[48:63], v[196:199], v[136:139], v[48:63]\n\t" \
  "ds_read_b64_tr_b16 v[196:197], v226 offset:45056\n\t" \
  "ds_read_b64_tr_b16 v[198:199], v226 offset:47104\n\t" \
  "v_add_f32_e32 v232, v232, v143\n\t" \
  "v_exp_f32_e32 v152, v152\n\t" \
  "v_exp_f32_e32 v153, v153\n\t" \
  "v_exp_f32_e32 v154, v154\n\t" \
  "v_exp_f32_e32 v155, v155\n\t" \
  "v_exp_f32_e32 v156, v156\n\t" \
  "v_exp_f32_e32 v157, v157\n\t" \
  "v_exp_f32_e32 v158, v158\n\t" \
  "v_exp_f32_e32 v159, v159\n\t" \
  "v_add_f32_e32 v231, v231, v144\n\t" \
  "v_add_f32_e32 v232, v232, v145\n\t" \
  "v_add_f32_e32 v231, v231, v146\n\t" \
  "v_add_f32_e32 v232, v232, v147\n\t" \
  "v_cvt_pk_bf16_f32 v144, v144, v145\n\t" \
  "v_add_f32_e32 v231, v231, v148\n\t" \
  "v_cvt_pk_bf16_f32 v145, v146, v147\n\t" \
  "v_add_f32_e32 v232, v232, v149\n\t" \
  "v_cvt_pk_bf16_f32 v146, v148, v149\n\t" \
  "v_add_f32_e32 v231, v231, v150\n\t" \
  "v_cvt_pk_bf16_f32 v147, v150, v151\n\t" \
  "s_waitcnt lgkmcnt(8)\n\t" \
  "s_nop 0\n\t" \
  "v_mfma_f32_32x32x16_bf16 v[0:15], v[200:203], v[144:147], v[0:15]\n\t" \
  "ds_read_b64_tr_b16 v[200:201], v227 offset:40960\n\t" \
  "ds_read_b64_tr_b16 v[202:203], v227 offset:43008\n\t" \
  "v_add_f32_e32 v232, v232, v151\n\t" \
  "v_add_f32_e32 v231, v231, v152\n\t" \
  "v_add_f32_e32 v232, v232, v153\n\t" \
  "v_add_f32_e32 v231, v231, v154\n\t" \
  "v_add_f32_e32 v232, v232, v155\n\t" \
  "v_cvt_pk_bf16_f32 v152, v152, v153\n\t" \
  "v_add_f32_e32 v231, v231, v156\n\t" \
  "v_cvt_pk_bf16_f32 v153, v154, v155\n\t" \
  "v_add_f32_e32 v232, v232, v157\n\t" \
  "v_cvt_pk_bf16_f32 v154, v156, v157\n\t" \
  "v_add_f32_e32 v231, v231, v158\n\t" \
  "v_cvt_pk_bf16_f32 v155, v158, v159\n\t" \
  "s_nop 1\n\t" \
  "v_mfma_f32_32x32x16_bf16 v[0:15], v[204:207], v[152:155], v[0:15]\n\t" \
  "ds_read_b64_tr_b16 v[204:205], v227 offset:45056\n\t" \
  "ds_read_b64_tr_b16 v[206:207], v227 offset:47104\n\t" \
  "v_add_f32_e32 v232, v232, v159\n\t" \
  "v_add_f32_e32 v231, v231, v232\n\t" \
  "v_mov_b32_e32 v235, v231\n\t" \
  "s_nop 1\n\t" \
  "v_permlane32_swap_b32_e32 v231, v235\n\t" \
  "v_add_f32_e32 v231, v231, v235\n\t" \
  "v_rcp_f32_e32 v238, v231\n\t" \
  "v_log_f32_e32 v239, v231\n\t" \
  "s_nop 0\n\t" \
  "v_add_f32_e32 v239, v239, v230\n\t" \
  "buffer_store_dword v239, v242, %[srd], 0 offen sc1\n\t" \
  "s_waitcnt lgkmcnt(8)\n\t" \
  "v_mfma_f32_32x32x16_bf16 v[16:31], v[208:211], v[144:147], v[16:31]\n\t" \
  "v_mul_f32_e32 v0, v0, v238\n\t" \
  "v_mul_f32_e32 v1, v1, v238\n\t" \
  "v_mul_f32_e32 v2, v2, v238\n\t" \
  "v_mul_f32_e32 v3, v3, v238\n\t" \
  "v_mul_f32_e32 v4, v4, v238\n\t" \
  "v_mul_f32_e32 v5, v5, v238\n\t" \
  "v_mul_f32_e32 v6, v6, v238\n\t" \
  "v_mul_f32_e32 v7, v7, v238\n\t" \
  "v_mul_f32_e32 v8, v8, v238\n\t" \
  "v_mul_f32_e32 v9, v9, v238\n\t" \
  "v_mul_f32_e32 v10, v10, v238\n\t" \
  "v_mul_f32_e32 v11, v11, v238\n\t" \
  "v_mul_f32_e32 v12, v12, v238\n\t" \
  "v_mul_f32_e32 v13, v13, v238\n\t" \
  "v_mfma_f32_32x32x16_bf16 v[16:31], v[212:215], v[152:155], v[16:31]\n\t" \
  "v_mul_f32_e32 v14, v14, v238\n\t" \
  "v_mul_f32_e32 v15, v15, v238\n\t" \
  "v_cvt_pk_bf16_f32 v0, v0, v1\n\t" \
  "v_cvt_pk_bf16_f32 v1, v2, v3\n\t" \
  "v_cvt_pk_bf16_f32 v2, v4, v5\n\t" \
  "v_cvt_pk_bf16_f32 v3, v6, v7\n\t" \
  "buffer_store_dwordx4 v[0:3], v240, %[srd], 0 offen offset:0 sc1\n\t" \
  "v_cvt_pk_bf16_f32 v8, v8, v9\n\t" \
  "v_cvt_pk_bf16_f32 v9, v10, v11\n\t" \
  "v_cvt_pk_bf16_f32 v10, v12, v13\n\t" \
  "v_cvt_pk_bf16_f32 v11, v14, v15\n\t" \
  "buffer_store_dwordx4 v[8:11], v240, %[srd], 0 offen offset:1024 sc1\n\t" \
  "v_mul_f32_e32 v16, v16, v238\n\t" \
  "v_mul_f32_e32 v17, v17, v238\n\t" \
  "s_waitcnt lgkmcnt(4)\n\t" \
  "v_mfma_f32_32x32x16_bf16 v[32:47], v[192:195], v[144:147], v[32:47]\n\t" \
  "v_mul_f32_e32 v18, v18, v238\n\t" \
  "v_mul_f32_e32 v19, v19, v238\n\t" \
  "v_mul_f32_e32 v20, v20, v238\n\t" \
  "v_mul_f32_e32 v21, v21, v238\n\t" \
  "v_mul_f32_e32 v22, v22, v238\n\t" \
  "v_mul_f32_e32 v23, v23, v238\n\t" \
  "v_mul_f32_e32 v24, v24, v238\n\t" \
  "v_mul_f32_e32 v25, v25, v238\n\t" \
  "v_mul_f32_e32 v26, v26, v238\n\t" \
  "v_mul_f32_e32 v27, v27, v238\n\t" \
  "v_mul_f32_e32 v28, v28, v238\n\t" \
  "v_mul_f32_e32 v29, v29, v238\n\t" \
  "v_mul_f32_e32 v30, v30, v238\n\t" \
  "v_mul_f32_e32 v31, v31, v238\n\t" \
  "v_mfma_f32_32x32x16_bf16 v[32:47], v[196:199], v[152:155], v[32:47]\n\t" \
  "v_cvt_pk_bf16_f32 v16, v16, v17\n\t" \
  "v_cvt_pk_bf16_f32 v17, v18, v19\n\t" \
  "v_cvt_pk_bf16_f32 v18, v20, v21\n\t" \
  "v_cvt_pk_bf16_f32 v19, v22, v23\n\t" \
  "buffer_store_dwordx4 v[16:19], v240, %[srd], 0 offen offset:2048 sc1\n\t" \
  "v_cvt_pk_bf16_f32 v24, v24, v25\n\t" \
  "v_cvt_pk_bf16_f32 v25, v26, v27\n\t" \
  "v_cvt_pk_bf16_f32 v26, v28, v29\n\t" \
  "v_cvt_pk_bf16_f32 v27, v30, v31\n\t" \
  "buffer_store_dwordx4 v[24:27], v240, %[srd], 0 offen offset:3072 sc1\n\t" \
  "s_nop 1\n\t" \
  "v_mul_f32_e32 v32, v32, v238\n\t" \
  "v_mul_f32_e32 v33, v33, v238\n\t" \
  "v_mul_f32_e32 v34, v34, v238\n\t" \
  "v_mul_f32_e32 v35, v35, v238\n\t" \
  "s_waitcnt lgkmcnt(0)\n\t" \
  "v_mfma_f32_32x32x16_bf16 v[48:63], v[200:203], v[144:147], v[48:63]\n\t" \
  "v_mul_f32_e32 v36, v36, v238\n\t" \
  "v_mul_f32_e32 v37, v37, v238\n\t" \
  "v_mul_f32_e32 v38, v38, v238\n\t" \
  "v_mul_f32_e32 v39, v39, v238\n\t" \
  "v_mul_f32_e32 v40, v40, v238\n\t" \
  "v_mul_f32_e32 v41, v41, v238\n\t" \
  "v_mul_f32_e32 v42, v42, v238\n\t" \
  "v_mul_f32_e32 v43, v43, v238\n\t" \
  "v_mul_f32_e32 v44, v44, v238\n\t" \
  "v_mul_f32_e32 v45, v45, v238\n\t" \
  "v_mul_f32_e32 v46, v46, v238\n\t" \
  "v_mul_f32_e32 v47, v47, v238\n\t" \
  "v_cvt_pk_bf16_f32 v32, v32, v33\n\t" \
  "v_cvt_pk_bf16_f32 v33, v34, v35\n\t" \
  "v_mfma_f32_32x32x16_bf16 v[48:63], v[204:207], v[152:155], v[48:63]\n\t" \
  "v_cvt_pk_bf16_f32 v34, v36, v37\n\t" \
  "v_cvt_pk_bf16_f32 v35, v38, v39\n\t" \
  "buffer_store_dwordx4 v[32:35], v241, %[srd], 0 offen offset:0 sc1\n\t" \
  "v_cvt_pk_bf16_f32 v40, v40, v41\n\t" \
  "v_cvt_pk_bf16_f32 v41, v42, v43\n\t" \
  "v_cvt_pk_bf16_f32 v42, v44, v45\n\t" \
  "v_cvt_pk_bf16_f32 v43, v46, v47\n\t" \
  "buffer_store_dwordx4 v[40:43], v241, %[srd], 0 offen offset:1024 sc1\n\t" \
  "s_nop 3\n\t" \
  "v_mul_f32_e32 v48, v48, v238\n\t" \
  "v_mul_f32_e32 v49, v49, v238\n\t" \
  "v_mul_f32_e32 v50, v50, v238\n\t" \
  "v_mul_f32_e32 v51, v51, v238\n\t" \
  "v_mul_f32_e32 v52, v52, v238\n\t" \
  "v_mul_f32_e32 v53, v53, v238\n\t" \
  "v_mul_f32_e32 v54, v54, v238\n\t" \
  "v_mul_f32_e32 v55, v55, v238\n\t" \
  "v_mul_f32_e32 v56, v56, v238\n\t" \
  "v_mul_f32_e32 v57, v57, v238\n\t" \
  "v_mul_f32_e32 v58, v58, v238\n\t" \
  "v_mul_f32_e32 v59, v59, v238\n\t" \
  "v_mul_f32_e32 v60, v60, v238\n\t" \
  "v_mul_f32_e32 v61, v61, v238\n\t" \
  "v_mul_f32_e32 v62, v62, v238\n\t" \
  "v_mul_f32_e32 v63, v63, v238\n\t" \
  "v_cvt_pk_bf16_f32 v48, v48, v49\n\t" \
  "v_cvt_pk_bf16_f32 v49, v50, v51\n\t" \
  "v_cvt_pk_bf16_f32 v50, v52, v53\n\t" \
  "v_cvt_pk_bf16_f32 v51, v54, v55\n\t" \
  "buffer_store_dwordx4 v[48:51], v241, %[srd], 0 offen offset:2048 sc1\n\t" \
  "v_cvt_pk_bf16_f32 v56, v56, v57\n\t" \
  "v_cvt_pk_bf16_f32 v57, v58, v59\n\t" \
  "v_cvt_pk_bf16_f32 v58, v60, v61\n\t" \
  "v_cvt_pk_bf16_f32 v59, v62, v63\n\t" \
  "buffer_store_dwordx4 v[56:59], v241, %[srd], 0 offen offset:3072 sc1\n\t" \
  "v_mov_b32_e32 v237, 25\n\t" \
  "ds_write_b32 v229, v237\n\t" \
  "FIN_%=:\n\t" \
  "s_waitcnt lgkmcnt(0)\n\t" \
  "s_branch END_%=\n\t" \
  "SPINP_%=:\n\t" \
  "s_mov_b32 %[ctr], 1048576\n\t" \
  "SPINLP_%=:\n\t" \
  "ds_read_b32 v236, v228\n\t" \
  "s_sub_u32 %[ctr], %[ctr], 1\n\t" \
  "s_waitcnt lgkmcnt(0)\n\t" \
  "v_cmp_gt_u32_e32 vcc, 11, v236\n\t" \
  "s_cbranch_scc1 BACKP_%=\n\t" \
  "s_cbranch_vccnz SPINLP_%=\n\t" \
  "s_branch BACKP_%=\n\t" \
  "SPINB0_%=:\n\t" \
  "s_mov_b32 %[ctr], 1048576\n\t" \
  "SPINLB0_%=:\n\t" \
  "ds_read_b32 v236, v228\n\t" \
  "s_sub_u32 %[ctr], %[ctr], 1\n\t" \
  "s_waitcnt lgkmcnt(0)\n\t" \
  "v_cmp_gt_u32_e32 vcc, 12, v236\n\t" \
  "s_cbranch_scc1 BACKB0_%=\n\t" \
  "s_cbranch_vccnz SPINLB0_%=\n\t" \
  "s_branch BACKB0_%=\n\t" \
  "SPINA1_%=:\n\t" \
  "s_mov_b32 %[ctr], 1048576\n\t" \
  "SPINLA1_%=:\n\t" \
  "ds_read_b32 v236, v228\n\t" \
  "s_sub_u32 %[ctr], %[ctr], 1\n\t" \
  "s_waitcnt lgkmcnt(0)\n\t" \
  "v_cmp_gt_u32_e32 vcc, 13, v236\n\t" \
  "s_cbranch_scc1 BACKA1_%=\n\t" \
  "s_cbranch_vccnz SPINLA1_%=\n\t" \
  "s_branch BACKA1_%=\n\t" \
  "SPINB1c_%=:\n\t" \
  "s_mov_b32 %[ctr], 1048576\n\t" \
  "SPINLB1c_%=:\n\t" \
  "ds_read_b32 v236, v228\n\t" \
  "s_sub_u32 %[ctr], %[ctr], 1\n\t" \
  "s_waitcnt lgkmcnt(0)\n\t" \
  "v_cmp_gt_u32_e32 vcc, 15, v236\n\t" \
  "s_cbranch_scc1 BACKB1c_%=\n\t" \
  "s_cbranch_vccnz SPINLB1c_%=\n\t" \
  "s_branch BACKB1c_%=\n\t" \
  "SPINB1r_%=:\n\t" \
  "s_mov_b32 %[ctr], 1048576\n\t" \
  "SPINLB1r_%=:\n\t" \
  "ds_read_b32 v236, v228\n\t" \
  "s_sub_u32 %[ctr], %[ctr], 1\n\t" \
  "s_waitcnt lgkmcnt(0)\n\t" \
  "v_cmp_gt_u32_e32 vcc, 15, v236\n\t" \
  "s_cbranch_scc1 BACKB1r_%=\n\t" \
  "s_cbranch_vccnz SPINLB1r_%=\n\t" \
  "s_branch BACKB1r_%=\n\t" \
  "RARE1_%=:\n\t" \
  "v_max_f32_e32 v238, 0, v233\n\t" \
  "v_add_f32_e32 v230, v230, v238\n\t" \
  "v_sub_f32_e32 v239, 0, v238\n\t" \
  "v_exp_f32_e32 v239, v239\n\t" \
  "v_sub_f32_e32 v128, v128, v238\n\t" \
  "v_sub_f32_e32 v129, v129, v238\n\t" \
  "v_sub_f32_e32 v130, v130, v238\n\t" \
  "v_sub_f32_e32 v131, v131, v238\n\t" \
  "v_sub_f32_e32 v132, v132, v238\n\t" \
  "v_sub_f32_e32 v133, v133, v238\n\t" \
  "v_sub_f32_e32 v134, v134, v238\n\t" \
  "v_sub_f32_e32 v135, v135, v238\n\t" \
  "v_sub_f32_e32 v136, v136, v238\n\t" \
  "v_sub_f32_e32 v137, v137, v238\n\t" \
  "v_sub_f32_e32 v138, v138, v238\n\t" \
  "v_sub_f32_e32 v139, v139, v238\n\t" \
  "v_sub_f32_e32 v140, v140, v238\n\t" \
  "v_sub_f32_e32 v141, v141, v238\n\t" \
  "v_sub_f32_e32 v142, v142, v238\n\t" \
  "v_sub_f32_e32 v143, v143, v238\n\t" \
  "v_sub_f32_e32 v144, v144, v238\n\t" \
  "v_sub_f32_e32 v145, v145, v238\n\t" \
  "v_sub_f32_e32 v146, v146, v238\n\t" \
  "v_sub_f32_e32 v147, v147, v238\n\t" \
  "v_sub_f32_e32 v148, v148, v238\n\t" \
  "v_sub_f32_e32 v149, v149, v238\n\t" \
  "v_sub_f32_e32 v150, v150, v238\n\t" \
  "v_sub_f32_e32 v151, v151, v238\n\t" \
  "v_sub_f32_e32 v152, v152, v238\n\t" \
  "v_sub_f32_e32 v153, v153, v238\n\t" \
  "v_sub_f32_e32 v154, v154, v238\n\t" \
  "v_sub_f32_e32 v155, v155, v238\n\t" \
  "v_sub_f32_e32 v156, v156, v238\n\t" \
  "v_sub_f32_e32 v157, v157, v238\n\t" \
  "v_sub_f32_e32 v158, v158, v238\n\t" \
  "v_sub_f32_e32 v159, v159, v238\n\t" \
  "v_sub_f32_e32 v160, 0, v230\n\t" \
  "v_sub_f32_e32 v161, 0, v230\n\t" \
  "v_sub_f32_e32 v162, 0, v230\n\t" \
  "v_sub_f32_e32 v163, 0, v230\n\t" \
  "v_sub_f32_e32 v164, 0, v230\n\t" \
  "v_sub_f32_e32 v165, 0, v230\n\t" \
  "v_sub_f32_e32 v166, 0, v230\n\t" \
  "v_sub_f32_e32 v167, 0, v230\n\t" \
  "v_sub_f32_e32 v168, 0, v230\n\t" \
  "v_sub_f32_e32 v169, 0, v230\n\t" \
  "v_sub_f32_e32 v170, 0, v230\n\t" \
  "v_sub_f32_e32 v171, 0, v230\n\t" \
  "v_sub_f32_e32 v172, 0, v230\n\t" \
  "v_sub_f32_e32 v173, 0, v230\n\t" \
  "v_sub_f32_e32 v174, 0, v230\n\t" \
  "v_sub_f32_e32 v175, 0, v230\n\t" \
  "v_mul_f32_e32 v231, v231, v239\n\t" \
  "v_mul_f32_e32 v232, v232, v239\n\t" \
  "v_mfma_f32_32x32x16_bf16 v[48:63], v[196:199], v[104:107], v[48:63]\n\t" \
  "ds_read_b64_tr_b16 v[196:197], v225 offset:12288\n\t" \
  "ds_read_b64_tr_b16 v[198:199], v225 offset:14336\n\t" \
  "v_exp_f32_e32 v128, v128\n\t" \
  "v_exp_f32_e32 v129, v129\n\t" \
  "s_waitcnt lgkmcnt(8)\n\t" \
  "v_mfma_f32_32x32x16_bf16 v[0:15], v[200:203], v[112:115], v[0:15]\n\t" \
  "ds_read_b64_tr_b16 v[200:201], v226 offset:12288\n\t" \
  "ds_read_b64_tr_b16 v[202:203], v226 offset:14336\n\t" \
  "v_exp_f32_e32 v130, v130\n\t" \
  "v_exp_f32_e32 v131, v131\n\t" \
  "v_mfma_f32_32x32x16_bf16 v[16:31], v[204:207], v[112:115], v[16:31]\n\t" \
  "ds_read_b64_tr_b16 v[204:205], v227 offset:12288\n\t" \
  "ds_read_b64_tr_b16 v[206:207], v227 offset:14336\n\t" \
  "ds_read_b32 v236, v228\n\t" \
  "v_exp_f32_e32 v132, v132\n\t" \
  "s_waitcnt lgkmcnt(9)\n\t" \
  "v_mfma_f32_32x32x16_bf16 v[32:47], v[208:211], v[112:115], v[32:47]\n\t" \
  "v_exp_f32_e32 v133, v133\n\t" \
  "v_exp_f32_e32 v134, v134\n\t" \
  "v_exp_f32_e32 v135, v135\n\t" \
  "v_mfma_f32_32x32x16_bf16 v[48:63], v[212:215], v[112:115], v[48:63]\n\t" \
  "s_waitcnt lgkmcnt(0)\n\t" \
  "v_cmp_gt_u32_e32 vcc, 15, v236\n\t" \
  "s_cbranch_vccnz SPINB1r_%=\n\t" \
  "BACKB1r_%=:\n\t" \
  "v_exp_f32_e32 v136, v136\n\t" \
  "v_exp_f32_e32 v137, v137\n\t" \
  "v_mfma_f32_32x32x16_bf16 v[0:15], v[192:195], v[120:123], v[0:15]\n\t" \
  "ds_read_b128 v[176:179], v216 offset:49152\n\t" \
  "v_exp_f32_e32 v138, v138\n\t" \
  "v_exp_f32_e32 v139, v139\n\t" \
  "v_mfma_f32_32x32x16_bf16 v[16:31], v[196:199], v[120:123], v[16:31]\n\t" \
  "ds_read_b128 v[180:183], v216 offset:57344\n\t" \
  "v_exp_f32_e32 v140, v140\n\t" \
  "v_exp_f32_e32 v141, v141\n\t" \
  "v_mfma_f32_32x32x16_bf16 v[32:47], v[200:203], v[120:123], v[32:47]\n\t" \
  "ds_read_b128 v[184:187], v217 offset:49152\n\t" \
  "v_exp_f32_e32 v142, v142\n\t" \
  "v_exp_f32_e32 v143, v143\n\t" \
  "v_add_f32_e32 v231, v231, v128\n\t" \
  "v_mfma_f32_32x32x16_bf16 v[48:63], v[204:207], v[120:123], v[48:63]\n\t" \
  "ds_read_b128 v[188:191], v217 offset:57344\n\t" \
  "v_add_f32_e32 v232, v232, v129\n\t" \
  "v_add_f32_e32 v231, v231, v130\n\t" \
  "v_add_f32_e32 v232, v232, v131\n\t" \
  "v_mov_b32_e32 v237, 11\n\t" \
  "ds_write_b32 v229, v237\n\t" \
  "v_mul_f32_e32 v0, v0, v239\n\t" \
  "v_mul_f32_e32 v1, v1, v239\n\t" \
  "v_mul_f32_e32 v2, v2, v239\n\t" \
  "v_mul_f32_e32 v3, v3, v239\n\t" \
  "v_mul_f32_e32 v4, v4, v239\n\t" \
  "v_mul_f32_e32 v5, v5, v239\n\t" \
  "v_mul_f32_e32 v6, v6, v239\n\t" \
  "v_mul_f32_e32 v7, v7, v239\n\t" \
  "v_mul_f32_e32 v8, v8, v239\n\t" \
  "v_mul_f32_e32 v9, v9, v239\n\t" \
  "v_mul_f32_e32 v10, v10, v239\n\t" \
  "v_mul_f32_e32 v11, v11, v239\n\t" \
  "v_mul_f32_e32 v12, v12, v239\n\t" \
  "v_mul_f32_e32 v13, v13, v239\n\t" \
  "v_mul_f32_e32 v14, v14, v239\n\t" \
  "v_mul_f32_e32 v15, v15, v239\n\t" \
  "v_mul_f32_e32 v16, v16, v239\n\t" \
  "v_mul_f32_e32 v17, v17, v239\n\t" \
  "v_mul_f32_e32 v18, v18, v239\n\t" \
  "v_mul_f32_e32 v19, v19, v239\n\t" \
  "v_mul_f32_e32 v20, v20, v239\n\t" \
  "v_mul_f32_e32 v21, v21, v239\n\t" \
  "v_mul_f32_e32 v22, v22, v239\n\t" \
  "v_mul_f32_e32 v23, v23, v239\n\t" \
  "v_mul_f32_e32 v24, v24, v239\n\t" \
  "v_mul_f32_e32 v25, v25, v239\n\t" \
  "v_mul_f32_e32 v26, v26, v239\n\t" \
  "v_mul_f32_e32 v27, v27, v239\n\t" \
  "v_mul_f32_e32 v28, v28, v239\n\t" \
  "v_mul_f32_e32 v29, v29, v239\n\t" \
  "v_mul_f32_e32 v30, v30, v239\n\t" \
  "v_mul_f32_e32 v31, v31, v239\n\t" \
  "v_mul_f32_e32 v32, v32, v239\n\t" \
  "v_mul_f32_e32 v33, v33, v239\n\t" \
  "v_mul_f32_e32 v34, v34, v239\n\t" \
  "v_mul_f32_e32 v35, v35, v239\n\t" \
  "v_mul_f32_e32 v36, v36, v239\n\t" \
  "v_mul_f32_e32 v37, v37, v239\n\t" \
  "v_mul_f32_e32 v38, v38, v239\n\t" \
  "v_mul_f32_e32 v39, v39, v239\n\t" \
  "v_mul_f32_e32 v40, v40, v239\n\t" \
  "v_mul_f32_e32 v41, v41, v239\n\t" \
  "v_mul_f32_e32 v42, v42, v239\n\t" \
  "v_mul_f32_e32 v43, v43, v239\n\t" \
  "v_mul_f32_e32 v44, v44, v239\n\t" \
  "v_mul_f32_e32 v45, v45, v239\n\t" \
  "v_mul_f32_e32 v46, v46, v239\n\t" \
  "v_mul_f32_e32 v47, v47, v239\n\t" \
  "v_mul_f32_e32 v48, v48, v239\n\t" \
  "v_mul_f32_e32 v49, v49, v239\n\t" \
  "v_mul_f32_e32 v50, v50, v239\n\t" \
  "v_mul_f32_e32 v51, v51, v239\n\t" \
  "v_mul_f32_e32 v52, v52, v239\n\t" \
  "v_mul_f32_e32 v53, v53, v239\n\t" \
  "v_mul_f32_e32 v54, v54, v239\n\t" \
  "v_mul_f32_e32 v55, v55, v239\n\t" \
  "v_mul_f32_e32 v56, v56, v239\n\t" \
  "v_mul_f32_e32 v57, v57, v239\n\t" \
  "v_mul_f32_e32 v58, v58, v239\n\t" \
  "v_mul_f32_e32 v59, v59, v239\n\t" \
  "v_mul_f32_e32 v60, v60, v239\n\t" \
  "v_mul_f32_e32 v61, v61, v239\n\t" \
  "v_mul_f32_e32 v62, v62, v239\n\t" \
  "v_mul_f32_e32 v63, v63, v239\n\t" \
  "s_branch NEXT1_%=\n\t" \
  "SPINB2c_%=:\n\t" \
  "s_mov_b32 %[ctr], 1048576\n\t" \
  "SPINLB2c_%=:\n\t" \
  "ds_read_b32 v236, v228\n\t" \
  "s_sub_u32 %[ctr], %[ctr], 1\n\t" \
  "s_waitcnt lgkmcnt(0)\n\t" \
  "v_cmp_gt_u32_e32 vcc, 16, v236\n\t" \
  "s_cbranch_scc1 BACKB2c_%=\n\t" \
  "s_cbranch_vccnz SPINLB2c_%=\n\t" \
  "s_branch BACKB2c_%=\n\t" \
  "SPINB2r_%=:\n\t" \
  "s_mov_b32 %[ctr], 1048576\n\t" \
  "SPINLB2r_%=:\n\t" \
  "ds_read_b32 v236, v228\n\t" \
  "s_sub_u32 %[ctr], %[ctr], 1\n\t" \
  "s_waitcnt lgkmcnt(0)\n\t" \
  "v_cmp_gt_u32_e32 vcc, 16, v236\n\t" \
  "s_cbranch_scc1 BACKB2r_%=\n\t" \
  "s_cbranch_vccnz SPINLB2r_%=\n\t" \
  "s_branch BACKB2r_%=\n\t" \
  "RARE2_%=:\n\t" \
  "v_max_f32_e32 v238, 0, v233\n\t" \
  "v_add_f32_e32 v230, v230, v238\n\t" \
  "v_sub_f32_e32 v239, 0, v238\n\t" \
  "v_exp_f32_e32 v239, v239\n\t" \
  "v_sub_f32_e32 v96, v96, v238\n\t" \
  "v_sub_f32_e32 v97, v97, v238\n\t" \
  "v_sub_f32_e32 v98, v98, v238\n\t" \
  "v_sub_f32_e32 v99, v99, v238\n\t" \
  "v_sub_f32_e32 v100, v100, v238\n\t" \
  "v_sub_f32_e32 v101, v101, v238\n\t" \
  "v_sub_f32_e32 v102, v102, v238\n\t" \
  "v_sub_f32_e32 v103, v103, v238\n\t" \
  "v_sub_f32_e32 v104, v104, v238\n\t" \
  "v_sub_f32_e32 v105, v105, v238\n\t" \
  "v_sub_f32_e32 v106, v106, v238\n\t" \
  "v_sub_f32_e32 v107, v107, v238\n\t" \
  "v_sub_f32_e32 v108, v108, v238\n\t" \
  "v_sub_f32_e32 v109, v109, v238\n\t" \
  "v_sub_f32_e32 v110, v110, v238\n\t" \
  "v_sub_f32_e32 v111, v111, v238\n\t" \
  "v_sub_f32_e32 v112, v112, v238\n\t" \
  "v_sub_f32_e32 v113, v113, v238\n\t" \
  "v_sub_f32_e32 v114, v114, v238\n\t" \
  "v_sub_f32_e32 v115, v115, v238\n\t" \
  "v_sub_f32_e32 v116, v116, v238\n\t" \
  "v_sub_f32_e32 v117, v117, v238\n\t" \
  "v_sub_f32_e32 v118, v118, v238\n\t" \
  "v_sub_f32_e32 v119, v119, v238\n\t" \
  "v_sub_f32_e32 v120, v120, v238\n\t" \
  "v_sub_f32_e32 v121, v121, v238\n\t" \
  "v_sub_f32_e32 v122, v122, v238\n\t" \
  "v_sub_f32_e32 v123, v123, v238\n\t" \
  "v_sub_f32_e32 v124, v124, v238\n\t" \
  "v_sub_f32_e32 v125, v125, v238\n\t" \
  "v_sub_f32_e32 v126, v126, v238\n\t" \
  "v_sub_f32_e32 v127, v127, v238\n\t" \
  "v_sub_f32_e32 v160, 0, v230\n\t" \
  "v_sub_f32_e32 v161, 0, v230\n\t" \
  "v_sub_f32_e32 v162, 0, v230\n\t" \
  "v_sub_f32_e32 v163, 0, v230\n\t" \
  "v_sub_f32_e32 v164, 0, v230\n\t" \
  "v_sub_f32_e32 v165, 0, v230\n\t" \
  "v_sub_f32_e32 v166, 0, v230\n\t" \
  "v_sub_f32_e32 v167, 0, v230\n\t" \
  "v_sub_f32_e32 v168, 0, v230\n\t" \
  "v_sub_f32_e32 v169, 0, v230\n\t" \
  "v_sub_f32_e32 v170, 0, v230\n\t" \
  "v_sub_f32_e32 v171, 0, v230\n\t" \
  "v_sub_f32_e32 v172, 0, v230\n\t" \
  "v_sub_f32_e32 v173, 0, v230\n\t" \
  "v_sub_f32_e32 v174, 0, v230\n\t" \
  "v_sub_f32_e32 v175, 0, v230\n\t" \
  "v_mul_f32_e32 v231, v231, v239\n\t" \
  "v_mul_f32_e32 v232, v232, v239\n\t" \
  "v_mfma_f32_32x32x16_bf16 v[48:63], v[196:199], v[136:139], v[48:63]\n\t" \
  "ds_read_b64_tr_b16 v[196:197], v225 offset:28672\n\t" \
  "ds_read_b64_tr_b16 v[198:199], v225 offset:30720\n\t" \
  "v_exp_f32_e32 v96, v96\n\t" \
  "v_exp_f32_e32 v97, v97\n\t" \
  "s_waitcnt lgkmcnt(8)\n\t" \
  "v_mfma_f32_32x32x16_bf16 v[0:15], v[200:203], v[144:147], v[0:15]\n\t" \
  "ds_read_b64_tr_b16 v[200:201], v226 offset:28672\n\t" \
  "ds_read_b64_tr_b16 v[202:203], v226 offset:30720\n\t" \
  "v_exp_f32_e32 v98, v98\n\t" \
  "v_exp_f32_e32 v99, v99\n\t" \
  "v_mfma_f32_32x32x16_bf16 v[16:31], v[204:207], v[144:147], v[16:31]\n\t" \
  "ds_read_b64_tr_b16 v[204:205], v227 offset:28672\n\t" \
  "ds_read_b64_tr_b16 v[206:207], v227 offset:30720\n\t" \
  "ds_read_b32 v236, v228\n\t" \
  "v_exp_f32_e32 v100, v100\n\t" \
  "s_waitcnt lgkmcnt(9)\n\t" \
  "v_mfma_f32_32x32x16_bf16 v[32:47], v[208:211], v[144:147], v[32:47]\n\t" \
  "v_exp_f32_e32 v101, v101\n\t" \
  "v_exp_f32_e32 v102, v102\n\t" \
  "v_exp_f32_e32 v103, v103\n\t" \
  "v_mfma_f32_32x32x16_bf16 v[48:63], v[212:215], v[144:147], v[48:63]\n\t" \
  "s_waitcnt lgkmcnt(0)\n\t" \
  "v_cmp_gt_u32_e32 vcc, 16, v236\n\t" \
  "s_cbranch_vccnz SPINB2r_%=\n\t" \
  "BACKB2r_%=:\n\t" \
  "v_exp_f32_e32 v104, v104\n\t" \
  "v_exp_f32_e32 v105, v105\n\t" \
  "v_mfma_f32_32x32x16_bf16 v[0:15], v[192:195], v[152:155], v[0:15]\n\t" \
  "ds_read_b128 v[176:179], v216 offset:0\n\t" \
  "v_exp_f32_e32 v106, v106\n\t" \
  "v_exp_f32_e32 v107, v107\n\t" \
  "v_mfma_f32_32x32x16_bf16 v[16:31], v[196:199], v[152:155], v[16:31]\n\t" \
  "ds_read_b128 v[180:183], v216 offset:8192\n\t" \
  "v_exp_f32_e32 v108, v108\n\t" \
  "v_exp_f32_e32 v109, v109\n\t" \
  "v_mfma_f32_32x32x16_bf16 v[32:47], v[200:203], v[152:155], v[32:47]\n\t" \
  "ds_read_b128 v[184:187], v217 offset:0\n\t" \
  "v_exp_f32_e32 v110, v110\n\t" \
  "v_exp_f32_e32 v111, v111\n\t" \
  "v_add_f32_e32 v231, v231, v96\n\t" \
  "v_mfma_f32_32x32x16_bf16 v[48:63], v[204:207], v[152:155], v[48:63]\n\t" \
  "ds_read_b128 v[188:191], v217 offset:8192\n\t" \
  "v_add_f32_e32 v232, v232, v97\n\t" \
  "v_add_f32_e32 v231, v231, v98\n\t" \
  "v_add_f32_e32 v232, v232, v99\n\t" \
  "v_mov_b32_e32 v237, 13\n\t" \
  "ds_write_b32 v229, v237\n\t" \
  "v_mul_f32_e32 v0, v0, v239\n\t" \
  "v_mul_f32_e32 v1, v1, v239\n\t" \
  "v_mul_f32_e32 v2, v2, v239\n\t" \
  "v_mul_f32_e32 v3, v3, v239\n\t" \
  "v_mul_f32_e32 v4, v4, v239\n\t" \
  "v_mul_f32_e32 v5, v5, v239\n\t" \
  "v_mul_f32_e32 v6, v6, v239\n\t" \
  "v_mul_f32_e32 v7, v7, v239\n\t" \
  "v_mul_f32_e32 v8, v8, v239\n\t" \
  "v_mul_f32_e32 v9, v9, v239\n\t" \
  "v_mul_f32_e32 v10, v10, v239\n\t" \
  "v_mul_f32_e32 v11, v11, v239\n\t" \
  "v_mul_f32_e32 v12, v12, v239\n\t" \
  "v_mul_f32_e32 v13, v13, v239\n\t" \
  "v_mul_f32_e32 v14, v14, v239\n\t" \
  "v_mul_f32_e32 v15, v15, v239\n\t" \
  "v_mul_f32_e32 v16, v16, v239\n\t" \
  "v_mul_f32_e32 v17, v17, v239\n\t" \
  "v_mul_f32_e32 v18, v18, v239\n\t" \
  "v_mul_f32_e32 v19, v19, v239\n\t" \
  "v_mul_f32_e32 v20, v20, v239\n\t" \
  "v_mul_f32_e32 v21, v21, v239\n\t" \
  "v_mul_f32_e32 v22, v22, v239\n\t" \
  "v_mul_f32_e32 v23, v23, v239\n\t" \
  "v_mul_f32_e32 v24, v24, v239\n\t" \
  "v_mul_f32_e32 v25, v25, v239\n\t" \
  "v_mul_f32_e32 v26, v26, v239\n\t" \
  "v_mul_f32_e32 v27, v27, v239\n\t" \
  "v_mul_f32_e32 v28, v28, v239\n\t" \
  "v_mul_f32_e32 v29, v29, v239\n\t" \
  "v_mul_f32_e32 v30, v30, v239\n\t" \
  "v_mul_f32_e32 v31, v31, v239\n\t" \
  "v_mul_f32_e32 v32, v32, v239\n\t" \
  "v_mul_f32_e32 v33, v33, v239\n\t" \
  "v_mul_f32_e32 v34, v34, v239\n\t" \
  "v_mul_f32_e32 v35, v35, v239\n\t" \
  "v_mul_f32_e32 v36, v36, v239\n\t" \
  "v_mul_f32_e32 v37, v37, v239\n\t" \
  "v_mul_f32_e32 v38, v38, v239\n\t" \
  "v_mul_f32_e32 v39, v39, v239\n\t" \
  "v_mul_f32_e32 v40, v40, v239\n\t" \
  "v_mul_f32_e32 v41, v41, v239\n\t" \
  "v_mul_f32_e32 v42, v42, v239\n\t" \
  "v_mul_f32_e32 v43, v43, v239\n\t" \
  "v_mul_f32_e32 v44, v44, v239\n\t" \
  "v_mul_f32_e32 v45, v45, v239\n\t" \
  "v_mul_f32_e32 v46, v46, v239\n\t" \
  "v_mul_f32_e32 v47, v47, v239\n\t" \
  "v_mul_f32_e32 v48, v48, v239\n\t" \
  "v_mul_f32_e32 v49, v49, v239\n\t" \
  "v_mul_f32_e32 v50, v50, v239\n\t" \
  "v_mul_f32_e32 v51, v51, v239\n\t" \
  "v_mul_f32_e32 v52, v52, v239\n\t" \
  "v_mul_f32_e32 v53, v53, v239\n\t" \
  "v_mul_f32_e32 v54, v54, v239\n\t" \
  "v_mul_f32_e32 v55, v55, v239\n\t" \
  "v_mul_f32_e32 v56, v56, v239\n\t" \
  "v_mul_f32_e32 v57, v57, v239\n\t" \
  "v_mul_f32_e32 v58, v58, v239\n\t" \
  "v_mul_f32_e32 v59, v59, v239\n\t" \
  "v_mul_f32_e32 v60, v60, v239\n\t" \
  "v_mul_f32_e32 v61, v61, v239\n\t" \
  "v_mul_f32_e32 v62, v62, v239\n\t" \
  "v_mul_f32_e32 v63, v63, v239\n\t" \
  "s_branch NEXT2_%=\n\t" \
  "SPINA3_%=:\n\t" \
  "s_mov_b32 %[ctr], 1048576\n\t" \
  "SPINLA3_%=:\n\t" \
  "ds_read_b32 v236, v228\n\t" \
  "s_sub_u32 %[ctr], %[ctr], 1\n\t" \
  "s_waitcnt lgkmcnt(0)\n\t" \
  "v_cmp_gt_u32_e32 vcc, 17, v236\n\t" \
  "s_cbranch_scc1 BACKA3_%=\n\t" \
  "s_cbranch_vccnz SPINLA3_%=\n\t" \
  "s_branch BACKA3_%=\n\t" \
  "SPINB3c_%=:\n\t" \
  "s_mov_b32 %[ctr], 1048576\n\t" \
  "SPINLB3c_%=:\n\t" \
  "ds_read_b32 v236, v228\n\t" \
  "s_sub_u32 %[ctr], %[ctr], 1\n\t" \
  "s_waitcnt lgkmcnt(0)\n\t" \
  "v_cmp_gt_u32_e32 vcc, 19, v236\n\t" \
  "s_cbranch_scc1 BACKB3c_%=\n\t" \
  "s_cbranch_vccnz SPINLB3c_%=\n\t" \
  "s_branch BACKB3c_%=\n\t" \
  "SPINB3r_%=:\n\t" \
  "s_mov_b32 %[ctr], 1048576\n\t" \
  "SPINLB3r_%=:\n\t" \
  "ds_read_b32 v236, v228\n\t" \
  "s_sub_u32 %[ctr], %[ctr], 1\n\t" \
  "s_waitcnt lgkmcnt(0)\n\t" \
  "v_cmp_gt_u32_e32 vcc, 19, v236\n\t" \
  "s_cbranch_scc1 BACKB3r_%=\n\t" \
  "s_cbranch_vccnz SPINLB3r_%=\n\t" \
  "s_branch BACKB3r_%=\n\t" \
  "RARE3_%=:\n\t" \
  "v_max_f32_e32 v238, 0, v233\n\t" \
  "v_add_f32_e32 v230, v230, v238\n\t" \
  "v_sub_f32_e32 v239, 0, v238\n\t" \
  "v_exp_f32_e32 v239, v239\n\t" \
  "v_sub_f32_e32 v128, v128, v238\n\t" \
  "v_sub_f32_e32 v129, v129, v238\n\t" \
  "v_sub_f32_e32 v130, v130, v238\n\t" \
  "v_sub_f32_e32 v131, v131, v238\n\t" \
  "v_sub_f32_e32 v132, v132, v238\n\t" \
  "v_sub_f32_e32 v133, v133, v238\n\t" \
  "v_sub_f32_e32 v134, v134, v238\n\t" \
  "v_sub_f32_e32 v135, v135, v238\n\t" \
  "v_sub_f32_e32 v136, v136, v238\n\t" \
  "v_sub_f32_e32 v137, v137, v238\n\t" \
  "v_sub_f32_e32 v138, v138, v238\n\t" \
  "v_sub_f32_e32 v139, v139, v238\n\t" \
  "v_sub_f32_e32 v140, v140, v238\n\t" \
  "v_sub_f32_e32 v141, v141, v238\n\t" \
  "v_sub_f32_e32 v142, v142, v238\n\t" \
  "v_sub_f32_e32 v143, v143, v238\n\t" \
  "v_sub_f32_e32 v144, v144, v238\n\t" \
  "v_sub_f32_e32 v145, v145, v238\n\t" \
  "v_sub_f32_e32 v146, v146, v238\n\t" \
  "v_sub_f32_e32 v147, v147, v238\n\t" \
  "v_sub_f32_e32 v148, v148, v238\n\t" \
  "v_sub_f32_e32 v149, v149, v238\n\t" \
  "v_sub_f32_e32 v150, v150, v238\n\t" \
  "v_sub_f32_e32 v151, v151, v238\n\t" \
  "v_sub_f32_e32 v152, v152, v238\n\t" \
  "v_sub_f32_e32 v153, v153, v238\n\t" \
  "v_sub_f32_e32 v154, v154, v238\n\t" \
  "v_sub_f32_e32 v155, v155, v238\n\t" \
  "v_sub_f32_e32 v156, v156, v238\n\t" \
  "v_sub_f32_e32 v157, v157, v238\n\t" \
  "v_sub_f32_e32 v158, v158, v238\n\t" \
  "v_sub_f32_e32 v159, v159, v238\n\t" \
  "v_sub_f32_e32 v160, 0, v230\n\t" \
  "v_sub_f32_e32 v161, 0, v230\n\t" \
  "v_sub_f32_e32 v162, 0, v230\n\t" \
  "v_sub_f32_e32 v163, 0, v230\n\t" \
  "v_sub_f32_e32 v164, 0, v230\n\t" \
  "v_sub_f32_e32 v165, 0, v230\n\t" \
  "v_sub_f32_e32 v166, 0, v230\n\t" \
  "v_sub_f32_e32 v167, 0, v230\n\t" \
  "v_sub_f32_e32 v168, 0, v230\n\t" \
  "v_sub_f32_e32 v169, 0, v230\n\t" \
  "v_sub_f32_e32 v170, 0, v230\n\t" \
  "v_sub_f32_e32 v171, 0, v230\n\t" \
  "v_sub_f32_e32 v172, 0, v230\n\t" \
  "v_sub_f32_e32 v173, 0, v230\n\t" \
  "v_sub_f32_e32 v174, 0, v230\n\t" \
  "v_sub_f32_e32 v175, 0, v230\n\t" \
  "v_mul_f32_e32 v231, v231, v239\n\t" \
  "v_mul_f32_e32 v232, v232, v239\n\t" \
  "v_mfma_f32_32x32x16_bf16 v[48:63], v[196:199], v[104:107], v[48:63]\n\t" \
  "ds_read_b64_tr_b16 v[196:197], v225 offset:45056\n\t" \
  "ds_read_b64_tr_b16 v[198:199], v225 offset:47104\n\t" \
  "v_exp_f32_e32 v128, v128\n\t" \
  "v_exp_f32_e32 v129, v129\n\t" \
  "s_waitcnt lgkmcnt(8)\n\t" \
  "v_mfma_f32_32x32x16_bf16 v[0:15], v[200:203], v[112:115], v[0:15]\n\t" \
  "ds_read_b64_tr_b16 v[200:201], v226 offset:45056\n\t" \
  "ds_read_b64_tr_b16 v[202:203], v226 offset:47104\n\t" \
  "v_exp_f32_e32 v130, v130\n\t" \
  "v_exp_f32_e32 v131, v131\n\t" \
  "v_mfma_f32_32x32x16_bf16 v[16:31], v[204:207], v[112:115], v[16:31]\n\t" \
  "ds_read_b64_tr_b16 v[204:205], v227 offset:45056\n\t" \
  "ds_read_b64_tr_b16 v[206:207], v227 offset:47104\n\t" \
  "ds_read_b32 v236, v228\n\t" \
  "v_exp_f32_e32 v132, v132\n\t" \
  "s_waitcnt lgkmcnt(9)\n\t" \
  "v_mfma_f32_32x32x16_bf16 v[32:47], v[208:211], v[112:115], v[32:47]\n\t" \
  "v_exp_f32_e32 v133, v133\n\t" \
  "v_exp_f32_e32 v134, v134\n\t" \
  "v_exp_f32_e32 v135, v135\n\t" \
  "v_mfma_f32_32x32x16_bf16 v[48:63], v[212:215], v[112:115], v[48:63]\n\t" \
  "s_waitcnt lgkmcnt(0)\n\t" \
  "v_cmp_gt_u32_e32 vcc, 19, v236\n\t" \
  "s_cbranch_vccnz SPINB3r_%=\n\t" \
  "BACKB3r_%=:\n\t" \
  "v_exp_f32_e32 v136, v136\n\t" \
  "v_exp_f32_e32 v137, v137\n\t" \
  "v_mfma_f32_32x32x16_bf16 v[0:15], v[192:195], v[120:123], v[0:15]\n\t" \
  "ds_read_b128 v[176:179], v216 offset:16384\n\t" \
  "v_exp_f32_e32 v138, v138\n\t" \
  "v_exp_f32_e32 v139, v139\n\t" \
  "v_mfma_f32_32x32x16_bf16 v[16:31], v[196:199], v[120:123], v[16:31]\n\t" \
  "ds_read_b128 v[180:183], v216 offset:24576\n\t" \
  "v_exp_f32_e32 v140, v140\n\t" \
  "v_exp_f32_e32 v141, v141\n\t" \
  "v_mfma_f32_32x32x16_bf16 v[32:47], v[200:203], v[120:123], v[32:47]\n\t" \
  "ds_read_b128 v[184:187], v217 offset:16384\n\t" \
  "v_exp_f32_e32 v142, v142\n\t" \
  "v_exp_f32_e32 v143, v143\n\t" \
  "v_add_f32_e32 v231, v231, v128\n\t" \
  "v_mfma_f32_32x32x16_bf16 v[48:63], v[204:207], v[120:123], v[48:63]\n\t" \
  "ds_read_b128 v[188:191], v217 offset:24576\n\t" \
  "v_add_f32_e32 v232, v232, v129\n\t" \
  "v_add_f32_e32 v231, v231, v130\n\t" \
  "v_add_f32_e32 v232, v232, v131\n\t" \
  "v_mov_b32_e32 v237, 15\n\t" \
  "ds_write_b32 v229, v237\n\t" \
  "v_mul_f32_e32 v0, v0, v239\n\t" \
  "v_mul_f32_e32 v1, v1, v239\n\t" \
  "v_mul_f32_e32 v2, v2, v239\n\t" \
  "v_mul_f32_e32 v3, v3, v239\n\t" \
  "v_mul_f32_e32 v4, v4, v239\n\t" \
  "v_mul_f32_e32 v5, v5, v239\n\t" \
  "v_mul_f32_e32 v6, v6, v239\n\t" \
  "v_mul_f32_e32 v7, v7, v239\n\t" \
  "v_mul_f32_e32 v8, v8, v239\n\t" \
  "v_mul_f32_e32 v9, v9, v239\n\t" \
  "v_mul_f32_e32 v10, v10, v239\n\t" \
  "v_mul_f32_e32 v11, v11, v239\n\t" \
  "v_mul_f32_e32 v12, v12, v239\n\t" \
  "v_mul_f32_e32 v13, v13, v239\n\t" \
  "v_mul_f32_e32 v14, v14, v239\n\t" \
  "v_mul_f32_e32 v15, v15, v239\n\t" \
  "v_mul_f32_e32 v16, v16, v239\n\t" \
  "v_mul_f32_e32 v17, v17, v239\n\t" \
  "v_mul_f32_e32 v18, v18, v239\n\t" \
  "v_mul_f32_e32 v19, v19, v239\n\t" \
  "v_mul_f32_e32 v20, v20, v239\n\t" \
  "v_mul_f32_e32 v21, v21, v239\n\t" \
  "v_mul_f32_e32 v22, v22, v239\n\t" \
  "v_mul_f32_e32 v23, v23, v239\n\t" \
  "v_mul_f32_e32 v24, v24, v239\n\t" \
  "v_mul_f32_e32 v25, v25, v239\n\t" \
  "v_mul_f32_e32 v26, v26, v239\n\t" \
  "v_mul_f32_e32 v27, v27, v239\n\t" \
  "v_mul_f32_e32 v28, v28, v239\n\t" \
  "v_mul_f32_e32 v29, v29, v239\n\t" \
  "v_mul_f32_e32 v30, v30, v239\n\t" \
  "v_mul_f32_e32 v31, v31, v239\n\t" \
  "v_mul_f32_e32 v32, v32, v239\n\t" \
  "v_mul_f32_e32 v33, v33, v239\n\t" \
  "v_mul_f32_e32 v34, v34, v239\n\t" \
  "v_mul_f32_e32 v35, v35, v239\n\t" \
  "v_mul_f32_e32 v36, v36, v239\n\t" \
  "v_mul_f32_e32 v37, v37, v239\n\t" \
  "v_mul_f32_e32 v38, v38, v239\n\t" \
  "v_mul_f32_e32 v39, v39, v239\n\t" \
  "v_mul_f32_e32 v40, v40, v239\n\t" \
  "v_mul_f32_e32 v41, v41, v239\n\t" \
  "v_mul_f32_e32 v42, v42, v239\n\t" \
  "v_mul_f32_e32 v43, v43, v239\n\t" \
  "v_mul_f32_e32 v44, v44, v239\n\t" \
  "v_mul_f32_e32 v45, v45, v239\n\t" \
  "v_mul_f32_e32 v46, v46, v239\n\t" \
  "v_mul_f32_e32 v47, v47, v239\n\t" \
  "v_mul_f32_e32 v48, v48, v239\n\t" \
  "v_mul_f32_e32 v49, v49, v239\n\t" \
  "v_mul_f32_e32 v50, v50, v239\n\t" \
  "v_mul_f32_e32 v51, v51, v239\n\t" \
  "v_mul_f32_e32 v52, v52, v239\n\t" \
  "v_mul_f32_e32 v53, v53, v239\n\t" \
  "v_mul_f32_e32 v54, v54, v239\n\t" \
  "v_mul_f32_e32 v55, v55, v239\n\t" \
  "v_mul_f32_e32 v56, v56, v239\n\t" \
  "v_mul_f32_e32 v57, v57, v239\n\t" \
  "v_mul_f32_e32 v58, v58, v239\n\t" \
  "v_mul_f32_e32 v59, v59, v239\n\t" \
  "v_mul_f32_e32 v60, v60, v239\n\t" \
  "v_mul_f32_e32 v61, v61, v239\n\t" \
  "v_mul_f32_e32 v62, v62, v239\n\t" \
  "v_mul_f32_e32 v63, v63, v239\n\t" \
  "s_branch NEXT3_%=\n\t" \
  "SPINB4c_%=:\n\t" \
  "s_mov_b32 %[ctr], 1048576\n\t" \
  "SPINLB4c_%=:\n\t" \
  "ds_read_b32 v236, v228\n\t" \
  "s_sub_u32 %[ctr], %[ctr], 1\n\t" \
  "s_waitcnt lgkmcnt(0)\n\t" \
  "v_cmp_gt_u32_e32 vcc, 20, v236\n\t" \
  "s_cbranch_scc1 BACKB4c_%=\n\t" \
  "s_cbranch_vccnz SPINLB4c_%=\n\t" \
  "s_branch BACKB4c_%=\n\t" \
  "SPINB4r_%=:\n\t" \
  "s_mov_b32 %[ctr], 1048576\n\t" \
  "SPINLB4r_%=:\n\t" \
  "ds_read_b32 v236, v228\n\t" \
  "s_sub_u32 %[ctr], %[ctr], 1\n\t" \
  "s_waitcnt lgkmcnt(0)\n\t" \
  "v_cmp_gt_u32_e32 vcc, 20, v236\n\t" \
  "s_cbranch_scc1 BACKB4r_%=\n\t" \
  "s_cbranch_vccnz SPINLB4r_%=\n\t" \
  "s_branch BACKB4r_%=\n\t" \
  "RARE4_%=:\n\t" \
  "v_max_f32_e32 v238, 0, v233\n\t" \
  "v_add_f32_e32 v230, v230, v238\n\t" \
  "v_sub_f32_e32 v239, 0, v238\n\t" \
  "v_exp_f32_e32 v239, v239\n\t" \
  "v_sub_f32_e32 v96, v96, v238\n\t" \
  "v_sub_f32_e32 v97, v97, v238\n\t" \
  "v_sub_f32_e32 v98, v98, v238\n\t" \
  "v_sub_f32_e32 v99, v99, v238\n\t" \
  "v_sub_f32_e32 v100, v100, v238\n\t" \
  "v_sub_f32_e32 v101, v101, v238\n\t" \
  "v_sub_f32_e32 v102, v102, v238\n\t" \
  "v_sub_f32_e32 v103, v103, v238\n\t" \
  "v_sub_f32_e32 v104, v104, v238\n\t" \
  "v_sub_f32_e32 v105, v105, v238\n\t" \
  "v_sub_f32_e32 v106, v106, v238\n\t" \
  "v_sub_f32_e32 v107, v107, v238\n\t" \
  "v_sub_f32_e32 v108, v108, v238\n\t" \
  "v_sub_f32_e32 v109, v109, v238\n\t" \
  "v_sub_f32_e32 v110, v110, v238\n\t" \
  "v_sub_f32_e32 v111, v111, v238\n\t" \
  "v_sub_f32_e32 v112, v112, v238\n\t" \
  "v_sub_f32_e32 v113, v113, v238\n\t" \
  "v_sub_f32_e32 v114, v114, v238\n\t" \
  "v_sub_f32_e32 v115, v115, v238\n\t" \
  "v_sub_f32_e32 v116, v116, v238\n\t" \
  "v_sub_f32_e32 v117, v117, v238\n\t" \
  "v_sub_f32_e32 v118, v118, v238\n\t" \
  "v_sub_f32_e32 v119, v119, v238\n\t" \
  "v_sub_f32_e32 v120, v120, v238\n\t" \
  "v_sub_f32_e32 v121, v121, v238\n\t" \
  "v_sub_f32_e32 v122, v122, v238\n\t" \
  "v_sub_f32_e32 v123, v123, v238\n\t" \
  "v_sub_f32_e32 v124, v124, v238\n\t" \
  "v_sub_f32_e32 v125, v125, v238\n\t" \
  "v_sub_f32_e32 v126, v126, v238\n\t" \
  "v_sub_f32_e32 v127, v127, v238\n\t" \
  "v_sub_f32_e32 v160, 0, v230\n\t" \
  "v_sub_f32_e32 v161, 0, v230\n\t" \
  "v_sub_f32_e32 v162, 0, v230\n\t" \
  "v_sub_f32_e32 v163, 0, v230\n\t" \
  "v_sub_f32_e32 v164, 0, v230\n\t" \
  "v_sub_f32_e32 v165, 0, v230\n\t" \
  "v_sub_f32_e32 v166, 0, v230\n\t" \
  "v_sub_f32_e32 v167, 0, v230\n\t" \
  "v_sub_f32_e32 v168, 0, v230\n\t" \
  "v_sub_f32_e32 v169, 0, v230\n\t" \
  "v_sub_f32_e32 v170, 0, v230\n\t" \
  "v_sub_f32_e32 v171, 0, v230\n\t" \
  "v_sub_f32_e32 v172, 0, v230\n\t" \
  "v_sub_f32_e32 v173, 0, v230\n\t" \
  "v_sub_f32_e32 v174, 0, v230\n\t" \
  "v_sub_f32_e32 v175, 0, v230\n\t" \
  "v_mul_f32_e32 v231, v231, v239\n\t" \
  "v_mul_f32_e32 v232, v232, v239\n\t" \
  "v_mfma_f32_32x32x16_bf16 v[48:63], v[196:199], v[136:139], v[48:63]\n\t" \
  "ds_read_b64_tr_b16 v[196:197], v225 offset:61440\n\t" \
  "ds_read_b64_tr_b16 v[198:199], v225 offset:63488\n\t" \
  "v_exp_f32_e32 v96, v96\n\t" \
  "v_exp_f32_e32 v97, v97\n\t" \
  "s_waitcnt lgkmcnt(8)\n\t" \
  "v_mfma_f32_32x32x16_bf16 v[0:15], v[200:203], v[144:147], v[0:15]\n\t" \
  "ds_read_b64_tr_b16 v[200:201], v226 offset:61440\n\t" \
  "ds_read_b64_tr_b16 v[202:203], v226 offset:63488\n\t" \
  "v_exp_f32_e32 v98, v98\n\t" \
  "v_exp_f32_e32 v99, v99\n\t" \
  "v_mfma_f32_32x32x16_bf16 v[16:31], v[204:207], v[144:147], v[16:31]\n\t" \
  "ds_read_b64_tr_b16 v[204:205], v227 offset:61440\n\t" \
  "ds_read_b64_tr_b16 v[206:207], v227 offset:63488\n\t" \
  "ds_read_b32 v236, v228\n\t" \
  "v_exp_f32_e32 v100, v100\n\t" \
  "s_waitcnt lgkmcnt(9)\n\t" \
  "v_mfma_f32_32x32x16_bf16 v[32:47], v[208:211], v[144:147], v[32:47]\n\t" \
  "v_exp_f32_e32 v101, v101\n\t" \
  "v_exp_f32_e32 v102, v102\n\t" \
  "v_exp_f32_e32 v103, v103\n\t" \
  "v_mfma_f32_32x32x16_bf16 v[48:63], v[212:215], v[144:147], v[48:63]\n\t" \
  "s_waitcnt lgkmcnt(0)\n\t" \
  "v_cmp_gt_u32_e32 vcc, 20, v236\n\t" \
  "s_cbranch_vccnz SPINB4r_%=\n\t" \
  "BACKB4r_%=:\n\t" \
  "v_exp_f32_e32 v104, v104\n\t" \
  "v_exp_f32_e32 v105, v105\n\t" \
  "v_mfma_f32_32x32x16_bf16 v[0:15], v[192:195], v[152:155], v[0:15]\n\t" \
  "ds_read_b128 v[176:179], v216 offset:32768\n\t" \
  "v_exp_f32_e32 v106, v106\n\t" \
  "v_exp_f32_e32 v107, v107\n\t" \
  "v_mfma_f32_32x32x16_bf16 v[16:31], v[196:199], v[152:155], v[16:31]\n\t" \
  "ds_read_b128 v[180:183], v216 offset:40960\n\t" \
  "v_exp_f32_e32 v108, v108\n\t" \
  "v_exp_f32_e32 v109, v109\n\t" \
  "v_mfma_f32_32x32x16_bf16 v[32:47], v[200:203], v[152:155], v[32:47]\n\t" \
  "ds_read_b128 v[184:187], v217 offset:32768\n\t" \
  "v_exp_f32_e32 v110, v110\n\t" \
  "v_exp_f32_e32 v111, v111\n\t" \
  "v_add_f32_e32 v231, v231, v96\n\t" \
  "v_mfma_f32_32x32x16_bf16 v[48:63], v[204:207], v[152:155], v[48:63]\n\t" \
  "ds_read_b128 v[188:191], v217 offset:40960\n\t" \
  "v_add_f32_e32 v232, v232, v97\n\t" \
  "v_add_f32_e32 v231, v231, v98\n\t" \
  "v_add_f32_e32 v232, v232, v99\n\t" \
  "v_mov_b32_e32 v237, 17\n\t" \
  "ds_write_b32 v229, v237\n\t" \
  "v_mul_f32_e32 v0, v0, v239\n\t" \
  "v_mul_f32_e32 v1, v1, v239\n\t" \
  "v_mul_f32_e32 v2, v2, v239\n\t" \
  "v_mul_f32_e32 v3, v3, v239\n\t" \
  "v_mul_f32_e32 v4, v4, v239\n\t" \
  "v_mul_f32_e32 v5, v5, v239\n\t" \
  "v_mul_f32_e32 v6, v6, v239\n\t" \
  "v_mul_f32_e32 v7, v7, v239\n\t" \
  "v_mul_f32_e32 v8, v8, v239\n\t" \
  "v_mul_f32_e32 v9, v9, v239\n\t" \
  "v_mul_f32_e32 v10, v10, v239\n\t" \
  "v_mul_f32_e32 v11, v11, v239\n\t" \
  "v_mul_f32_e32 v12, v12, v239\n\t" \
  "v_mul_f32_e32 v13, v13, v239\n\t" \
  "v_mul_f32_e32 v14, v14, v239\n\t" \
  "v_mul_f32_e32 v15, v15, v239\n\t" \
  "v_mul_f32_e32 v16, v16, v239\n\t" \
  "v_mul_f32_e32 v17, v17, v239\n\t" \
  "v_mul_f32_e32 v18, v18, v239\n\t" \
  "v_mul_f32_e32 v19, v19, v239\n\t" \
  "v_mul_f32_e32 v20, v20, v239\n\t" \
  "v_mul_f32_e32 v21, v21, v239\n\t" \
  "v_mul_f32_e32 v22, v22, v239\n\t" \
  "v_mul_f32_e32 v23, v23, v239\n\t" \
  "v_mul_f32_e32 v24, v24, v239\n\t" \
  "v_mul_f32_e32 v25, v25, v239\n\t" \
  "v_mul_f32_e32 v26, v26, v239\n\t" \
  "v_mul_f32_e32 v27, v27, v239\n\t" \
  "v_mul_f32_e32 v28, v28, v239\n\t" \
  "v_mul_f32_e32 v29, v29, v239\n\t" \
  "v_mul_f32_e32 v30, v30, v239\n\t" \
  "v_mul_f32_e32 v31, v31, v239\n\t" \
  "v_mul_f32_e32 v32, v32, v239\n\t" \
  "v_mul_f32_e32 v33, v33, v239\n\t" \
  "v_mul_f32_e32 v34, v34, v239\n\t" \
  "v_mul_f32_e32 v35, v35, v239\n\t" \
  "v_mul_f32_e32 v36, v36, v239\n\t" \
  "v_mul_f32_e32 v37, v37, v239\n\t" \
  "v_mul_f32_e32 v38, v38, v239\n\t" \
  "v_mul_f32_e32 v39, v39, v239\n\t" \
  "v_mul_f32_e32 v40, v40, v239\n\t" \
  "v_mul_f32_e32 v41, v41, v239\n\t" \
  "v_mul_f32_e32 v42, v42, v239\n\t" \
  "v_mul_f32_e32 v43, v43, v239\n\t" \
  "v_mul_f32_e32 v44, v44, v239\n\t" \
  "v_mul_f32_e32 v45, v45, v239\n\t" \
  "v_mul_f32_e32 v46, v46, v239\n\t" \
  "v_mul_f32_e32 v47, v47, v239\n\t" \
  "v_mul_f32_e32 v48, v48, v239\n\t" \
  "v_mul_f32_e32 v49, v49, v239\n\t" \
  "v_mul_f32_e32 v50, v50, v239\n\t" \
  "v_mul_f32_e32 v51, v51, v239\n\t" \
  "v_mul_f32_e32 v52, v52, v239\n\t" \
  "v_mul_f32_e32 v53, v53, v239\n\t" \
  "v_mul_f32_e32 v54, v54, v239\n\t" \
  "v_mul_f32_e32 v55, v55, v239\n\t" \
  "v_mul_f32_e32 v56, v56, v239\n\t" \
  "v_mul_f32_e32 v57, v57, v239\n\t" \
  "v_mul_f32_e32 v58, v58, v239\n\t" \
  "v_mul_f32_e32 v59, v59, v239\n\t" \
  "v_mul_f32_e32 v60, v60, v239\n\t" \
  "v_mul_f32_e32 v61, v61, v239\n\t" \
  "v_mul_f32_e32 v62, v62, v239\n\t" \
  "v_mul_f32_e32 v63, v63, v239\n\t" \
  "s_branch NEXT4_%=\n\t" \
  "SPINA5_%=:\n\t" \
  "s_mov_b32 %[ctr], 1048576\n\t" \
  "SPINLA5_%=:\n\t" \
  "ds_read_b32 v236, v228\n\t" \
  "s_sub_u32 %[ctr], %[ctr], 1\n\t" \
  "s_waitcnt lgkmcnt(0)\n\t" \
  "v_cmp_gt_u32_e32 vcc, 21, v236\n\t" \
  "s_cbranch_scc1 BACKA5_%=\n\t" \
  "s_cbranch_vccnz SPINLA5_%=\n\t" \
  "s_branch BACKA5_%=\n\t" \
  "SPINB5c_%=:\n\t" \
  "s_mov_b32 %[ctr], 1048576\n\t" \
  "SPINLB5c_%=:\n\t" \
  "ds_read_b32 v236, v228\n\t" \
  "s_sub_u32 %[ctr], %[ctr], 1\n\t" \
  "s_waitcnt lgkmcnt(0)\n\t" \
  "v_cmp_gt_u32_e32 vcc, 23, v236\n\t" \
  "s_cbranch_scc1 BACKB5c_%=\n\t" \
  "s_cbranch_vccnz SPINLB5c_%=\n\t" \
  "s_branch BACKB5c_%=\n\t" \
  "SPINB5r_%=:\n\t" \
  "s_mov_b32 %[ctr], 1048576\n\t" \
  "SPINLB5r_%=:\n\t" \
  "ds_read_b32 v236, v228\n\t" \
  "s_sub_u32 %[ctr], %[ctr], 1\n\t" \
  "s_waitcnt lgkmcnt(0)\n\t" \
  "v_cmp_gt_u32_e32 vcc, 23, v236\n\t" \
  "s_cbranch_scc1 BACKB5r_%=\n\t" \
  "s_cbranch_vccnz SPINLB5r_%=\n\t" \
  "s_branch BACKB5r_%=\n\t" \
  "RARE5_%=:\n\t" \
  "v_max_f32_e32 v238, 0, v233\n\t" \
  "v_add_f32_e32 v230, v230, v238\n\t" \
  "v_sub_f32_e32 v239, 0, v238\n\t" \
  "v_exp_f32_e32 v239, v239\n\t" \
  "v_sub_f32_e32 v128, v128, v238\n\t" \
  "v_sub_f32_e32 v129, v129, v238\n\t" \
  "v_sub_f32_e32 v130, v130, v238\n\t" \
  "v_sub_f32_e32 v131, v131, v238\n\t" \
  "v_sub_f32_e32 v132, v132, v238\n\t" \
  "v_sub_f32_e32 v133, v133, v238\n\t" \
  "v_sub_f32_e32 v134, v134, v238\n\t" \
  "v_sub_f32_e32 v135, v135, v238\n\t" \
  "v_sub_f32_e32 v136, v136, v238\n\t" \
  "v_sub_f32_e32 v137, v137, v238\n\t" \
  "v_sub_f32_e32 v138, v138, v238\n\t" \
  "v_sub_f32_e32 v139, v139, v238\n\t" \
  "v_sub_f32_e32 v140, v140, v238\n\t" \
  "v_sub_f32_e32 v141, v141, v238\n\t" \
  "v_sub_f32_e32 v142, v142, v238\n\t" \
  "v_sub_f32_e32 v143, v143, v238\n\t" \
  "v_sub_f32_e32 v144, v144, v238\n\t" \
  "v_sub_f32_e32 v145, v145, v238\n\t" \
  "v_sub_f32_e32 v146, v146, v238\n\t" \
  "v_sub_f32_e32 v147, v147, v238\n\t" \
  "v_sub_f32_e32 v148, v148, v238\n\t" \
  "v_sub_f32_e32 v149, v149, v238\n\t" \
  "v_sub_f32_e32 v150, v150, v238\n\t" \
  "v_sub_f32_e32 v151, v151, v238\n\t" \
  "v_sub_f32_e32 v152, v152, v238\n\t" \
  "v_sub_f32_e32 v153, v153, v238\n\t" \
  "v_sub_f32_e32 v154, v154, v238\n\t" \
  "v_sub_f32_e32 v155, v155, v238\n\t" \
  "v_sub_f32_e32 v156, v156, v238\n\t" \
  "v_sub_f32_e32 v157, v157, v238\n\t" \
  "v_sub_f32_e32 v158, v158, v238\n\t" \
  "v_sub_f32_e32 v159, v159, v238\n\t" \
  "v_sub_f32_e32 v160, 0, v230\n\t" \
  "v_sub_f32_e32 v161, 0, v230\n\t" \
  "v_sub_f32_e32 v162, 0, v230\n\t" \
  "v_sub_f32_e32 v163, 0, v230\n\t" \
  "v_sub_f32_e32 v164, 0, v230\n\t" \
  "v_sub_f32_e32 v165, 0, v230\n\t" \
  "v_sub_f32_e32 v166, 0, v230\n\t" \
  "v_sub_f32_e32 v167, 0, v230\n\t" \
  "v_sub_f32_e32 v168, 0, v230\n\t" \
  "v_sub_f32_e32 v169, 0, v230\n\t" \
  "v_sub_f32_e32 v170, 0, v230\n\t" \
  "v_sub_f32_e32 v171, 0, v230\n\t" \
  "v_sub_f32_e32 v172, 0, v230\n\t" \
  "v_sub_f32_e32 v173, 0, v230\n\t" \
  "v_sub_f32_e32 v174, 0, v230\n\t" \
  "v_sub_f32_e32 v175, 0, v230\n\t" \
  "v_mul_f32_e32 v231, v231, v239\n\t" \
  "v_mul_f32_e32 v232, v232, v239\n\t" \
  "v_mfma_f32_32x32x16_bf16 v[48:63], v[196:199], v[104:107], v[48:63]\n\t" \
  "ds_read_b64_tr_b16 v[196:197], v245 offset:12288\n\t" \
  "ds_read_b64_tr_b16 v[198:199], v245 offset:14336\n\t" \
  "v_exp_f32_e32 v128, v128\n\t" \
  "v_exp_f32_e32 v129, v129\n\t" \
  "s_waitcnt lgkmcnt(8)\n\t" \
  "v_mfma_f32_32x32x16_bf16 v[0:15], v[200:203], v[112:115], v[0:15]\n\t" \
  "ds_read_b64_tr_b16 v[200:201], v246 offset:12288\n\t" \
  "ds_read_b64_tr_b16 v[202:203], v246 offset:14336\n\t" \
  "v_exp_f32_e32 v130, v130\n\t" \
  "v_exp_f32_e32 v131, v131\n\t" \
  "v_mfma_f32_32x32x16_bf16 v[16:31], v[204:207], v[112:115], v[16:31]\n\t" \
  "ds_read_b64_tr_b16 v[204:205], v247 offset:12288\n\t" \
  "ds_read_b64_tr_b16 v[206:207], v247 offset:14336\n\t" \
  "ds_read_b32 v236, v228\n\t" \
  "v_exp_f32_e32 v132, v132\n\t" \
  "s_waitcnt lgkmcnt(9)\n\t" \
  "v_mfma_f32_32x32x16_bf16 v[32:47], v[208:211], v[112:115], v[32:47]\n\t" \
  "v_exp_f32_e32 v133, v133\n\t" \
  "v_exp_f32_e32 v134, v134\n\t" \
  "v_exp_f32_e32 v135, v135\n\t" \
  "v_mfma_f32_32x32x16_bf16 v[48:63], v[212:215], v[112:115], v[48:63]\n\t" \
  "s_waitcnt lgkmcnt(0)\n\t" \
  "v_cmp_gt_u32_e32 vcc, 23, v236\n\t" \
  "s_cbranch_vccnz SPINB5r_%=\n\t" \
  "BACKB5r_%=:\n\t" \
  "v_exp_f32_e32 v136, v136\n\t" \
  "v_exp_f32_e32 v137, v137\n\t" \
  "v_mfma_f32_32x32x16_bf16 v[0:15], v[192:195], v[120:123], v[0:15]\n\t" \
  "ds_read_b128 v[176:179], v216 offset:49152\n\t" \
  "v_exp_f32_e32 v138, v138\n\t" \
  "v_exp_f32_e32 v139, v139\n\t" \
  "v_mfma_f32_32x32x16_bf16 v[16:31], v[196:199], v[120:123], v[16:31]\n\t" \
  "ds_read_b128 v[180:183], v216 offset:57344\n\t" \
  "v_exp_f32_e32 v140, v140\n\t" \
  "v_exp_f32_e32 v141, v141\n\t" \
  "v_mfma_f32_32x32x16_bf16 v[32:47], v[200:203], v[120:123], v[32:47]\n\t" \
  "ds_read_b128 v[184:187], v217 offset:49152\n\t" \
  "v_exp_f32_e32 v142, v142\n\t" \
  "v_exp_f32_e32 v143, v143\n\t" \
  "v_add_f32_e32 v231, v231, v128\n\t" \
  "v_mfma_f32_32x32x16_bf16 v[48:63], v[204:207], v[120:123], v[48:63]\n\t" \
  "ds_read_b128 v[188:191], v217 offset:57344\n\t" \
  "v_add_f32_e32 v232, v232, v129\n\t" \
  "v_add_f32_e32 v231, v231, v130\n\t" \
  "v_add_f32_e32 v232, v232, v131\n\t" \
  "v_mov_b32_e32 v237, 19\n\t" \
  "ds_write_b32 v229, v237\n\t" \
  "v_mul_f32_e32 v0, v0, v239\n\t" \
  "v_mul_f32_e32 v1, v1, v239\n\t" \
  "v_mul_f32_e32 v2, v2, v239\n\t" \
  "v_mul_f32_e32 v3, v3, v239\n\t" \
  "v_mul_f32_e32 v4, v4, v239\n\t" \
  "v_mul_f32_e32 v5, v5, v239\n\t" \
  "v_mul_f32_e32 v6, v6, v239\n\t" \
  "v_mul_f32_e32 v7, v7, v239\n\t" \
  "v_mul_f32_e32 v8, v8, v239\n\t" \
  "v_mul_f32_e32 v9, v9, v239\n\t" \
  "v_mul_f32_e32 v10, v10, v239\n\t" \
  "v_mul_f32_e32 v11, v11, v239\n\t" \
  "v_mul_f32_e32 v12, v12, v239\n\t" \
  "v_mul_f32_e32 v13, v13, v239\n\t" \
  "v_mul_f32_e32 v14, v14, v239\n\t" \
  "v_mul_f32_e32 v15, v15, v239\n\t" \
  "v_mul_f32_e32 v16, v16, v239\n\t" \
  "v_mul_f32_e32 v17, v17, v239\n\t" \
  "v_mul_f32_e32 v18, v18, v239\n\t" \
  "v_mul_f32_e32 v19, v19, v239\n\t" \
  "v_mul_f32_e32 v20, v20, v239\n\t" \
  "v_mul_f32_e32 v21, v21, v239\n\t" \
  "v_mul_f32_e32 v22, v22, v239\n\t" \
  "v_mul_f32_e32 v23, v23, v239\n\t" \
  "v_mul_f32_e32 v24, v24, v239\n\t" \
  "v_mul_f32_e32 v25, v25, v239\n\t" \
  "v_mul_f32_e32 v26, v26, v239\n\t" \
  "v_mul_f32_e32 v27, v27, v239\n\t" \
  "v_mul_f32_e32 v28, v28, v239\n\t" \
  "v_mul_f32_e32 v29, v29, v239\n\t" \
  "v_mul_f32_e32 v30, v30, v239\n\t" \
  "v_mul_f32_e32 v31, v31, v239\n\t" \
  "v_mul_f32_e32 v32, v32, v239\n\t" \
  "v_mul_f32_e32 v33, v33, v239\n\t" \
  "v_mul_f32_e32 v34, v34, v239\n\t" \
  "v_mul_f32_e32 v35, v35, v239\n\t" \
  "v_mul_f32_e32 v36, v36, v239\n\t" \
  "v_mul_f32_e32 v37, v37, v239\n\t" \
  "v_mul_f32_e32 v38, v38, v239\n\t" \
  "v_mul_f32_e32 v39, v39, v239\n\t" \
  "v_mul_f32_e32 v40, v40, v239\n\t" \
  "v_mul_f32_e32 v41, v41, v239\n\t" \
  "v_mul_f32_e32 v42, v42, v239\n\t" \
  "v_mul_f32_e32 v43, v43, v239\n\t" \
  "v_mul_f32_e32 v44, v44, v239\n\t" \
  "v_mul_f32_e32 v45, v45, v239\n\t" \
  "v_mul_f32_e32 v46, v46, v239\n\t" \
  "v_mul_f32_e32 v47, v47, v239\n\t" \
  "v_mul_f32_e32 v48, v48, v239\n\t" \
  "v_mul_f32_e32 v49, v49, v239\n\t" \
  "v_mul_f32_e32 v50, v50, v239\n\t" \
  "v_mul_f32_e32 v51, v51, v239\n\t" \
  "v_mul_f32_e32 v52, v52, v239\n\t" \
  "v_mul_f32_e32 v53, v53, v239\n\t" \
  "v_mul_f32_e32 v54, v54, v239\n\t" \
  "v_mul_f32_e32 v55, v55, v239\n\t" \
  "v_mul_f32_e32 v56, v56, v239\n\t" \
  "v_mul_f32_e32 v57, v57, v239\n\t" \
  "v_mul_f32_e32 v58, v58, v239\n\t" \
  "v_mul_f32_e32 v59, v59, v239\n\t" \
  "v_mul_f32_e32 v60, v60, v239\n\t" \
  "v_mul_f32_e32 v61, v61, v239\n\t" \
  "v_mul_f32_e32 v62, v62, v239\n\t" \
  "v_mul_f32_e32 v63, v63, v239\n\t" \
  "s_branch NEXT5_%=\n\t" \
  "SPINB6c_%=:\n\t" \
  "s_mov_b32 %[ctr], 1048576\n\t" \
  "SPINLB6c_%=:\n\t" \
  "ds_read_b32 v236, v228\n\t" \
  "s_sub_u32 %[ctr], %[ctr], 1\n\t" \
  "s_waitcnt lgkmcnt(0)\n\t" \
  "v_cmp_gt_u32_e32 vcc, 24, v236\n\t" \
  "s_cbranch_scc1 BACKB6c_%=\n\t" \
  "s_cbranch_vccnz SPINLB6c_%=\n\t" \
  "s_branch BACKB6c_%=\n\t" \
  "SPINB6r_%=:\n\t" \
  "s_mov_b32 %[ctr], 1048576\n\t" \
  "SPINLB6r_%=:\n\t" \
  "ds_read_b32 v236, v228\n\t" \
  "s_sub_u32 %[ctr], %[ctr], 1\n\t" \
  "s_waitcnt lgkmcnt(0)\n\t" \
  "v_cmp_gt_u32_e32 vcc, 24, v236\n\t" \
  "s_cbranch_scc1 BACKB6r_%=\n\t" \
  "s_cbranch_vccnz SPINLB6r_%=\n\t" \
  "s_branch BACKB6r_%=\n\t" \
  "RARE6_%=:\n\t" \
  "v_max_f32_e32 v238, 0, v233\n\t" \
  "v_add_f32_e32 v230, v230, v238\n\t" \
  "v_sub_f32_e32 v239, 0, v238\n\t" \
  "v_exp_f32_e32 v239, v239\n\t" \
  "v_sub_f32_e32 v96, v96, v238\n\t" \
  "v_sub_f32_e32 v97, v97, v238\n\t" \
  "v_sub_f32_e32 v98, v98, v238\n\t" \
  "v_sub_f32_e32 v99, v99, v238\n\t" \
  "v_sub_f32_e32 v100, v100, v238\n\t" \
  "v_sub_f32_e32 v101, v101, v238\n\t" \
  "v_sub_f32_e32 v102, v102, v238\n\t" \
  "v_sub_f32_e32 v103, v103, v238\n\t" \
  "v_sub_f32_e32 v104, v104, v238\n\t" \
  "v_sub_f32_e32 v105, v105, v238\n\t" \
  "v_sub_f32_e32 v106, v106, v238\n\t" \
  "v_sub_f32_e32 v107, v107, v238\n\t" \
  "v_sub_f32_e32 v108, v108, v238\n\t" \
  "v_sub_f32_e32 v109, v109, v238\n\t" \
  "v_sub_f32_e32 v110, v110, v238\n\t" \
  "v_sub_f32_e32 v111, v111, v238\n\t" \
  "v_sub_f32_e32 v112, v112, v238\n\t" \
  "v_sub_f32_e32 v113, v113, v238\n\t" \
  "v_sub_f32_e32 v114, v114, v238\n\t" \
  "v_sub_f32_e32 v115, v115, v238\n\t" \
  "v_sub_f32_e32 v116, v116, v238\n\t" \
  "v_sub_f32_e32 v117, v117, v238\n\t" \
  "v_sub_f32_e32 v118, v118, v238\n\t" \
  "v_sub_f32_e32 v119, v119, v238\n\t" \
  "v_sub_f32_e32 v120, v120, v238\n\t" \
  "v_sub_f32_e32 v121, v121, v238\n\t" \
  "v_sub_f32_e32 v122, v122, v238\n\t" \
  "v_sub_f32_e32 v123, v123, v238\n\t" \
  "v_sub_f32_e32 v124, v124, v238\n\t" \
  "v_sub_f32_e32 v125, v125, v238\n\t" \
  "v_sub_f32_e32 v126, v126, v238\n\t" \
  "v_sub_f32_e32 v127, v127, v238\n\t" \
  "v_sub_f32_e32 v160, 0, v230\n\t" \
  "v_sub_f32_e32 v161, 0, v230\n\t" \
  "v_sub_f32_e32 v162, 0, v230\n\t" \
  "v_sub_f32_e32 v163, 0, v230\n\t" \
  "v_sub_f32_e32 v164, 0, v230\n\t" \
  "v_sub_f32_e32 v165, 0, v230\n\t" \
  "v_sub_f32_e32 v166, 0, v230\n\t" \
  "v_sub_f32_e32 v167, 0, v230\n\t" \
  "v_sub_f32_e32 v168, 0, v230\n\t" \
  "v_sub_f32_e32 v169, 0, v230\n\t" \
  "v_sub_f32_e32 v170, 0, v230\n\t" \
  "v_sub_f32_e32 v171, 0, v230\n\t" \
  "v_sub_f32_e32 v172, 0, v230\n\t" \
  "v_sub_f32_e32 v173, 0, v230\n\t" \
  "v_sub_f32_e32 v174, 0, v230\n\t" \
  "v_sub_f32_e32 v175, 0, v230\n\t" \
  "v_mul_f32_e32 v231, v231, v239\n\t" \
  "v_mul_f32_e32 v232, v232, v239\n\t" \
  "v_mfma_f32_32x32x16_bf16 v[48:63], v[196:199], v[136:139], v[48:63]\n\t" \
  "ds_read_b64_tr_b16 v[196:197], v225 offset:12288\n\t" \
  "ds_read_b64_tr_b16 v[198:199], v225 offset:14336\n\t" \
  "v_exp_f32_e32 v96, v96\n\t" \
  "v_exp_f32_e32 v97, v97\n\t" \
  "s_waitcnt lgkmcnt(8)\n\t" \
  "v_mfma_f32_32x32x16_bf16 v[0:15], v[200:203], v[144:147], v[0:15]\n\t" \
  "ds_read_b64_tr_b16 v[200:201], v226 offset:12288\n\t" \
  "ds_read_b64_tr_b16 v[202:203], v226 offset:14336\n\t" \
  "v_exp_f32_e32 v98, v98\n\t" \
  "v_exp_f32_e32 v99, v99\n\t" \
  "v_mfma_f32_32x32x16_bf16 v[16:31], v[204:207], v[144:147], v[16:31]\n\t" \
  "ds_read_b64_tr_b16 v[204:205], v227 offset:12288\n\t" \
  "ds_read_b64_tr_b16 v[206:207], v227 offset:14336\n\t" \
  "ds_read_b32 v236, v228\n\t" \
  "v_exp_f32_e32 v100, v100\n\t" \
  "s_waitcnt lgkmcnt(9)\n\t" \
  "v_mfma_f32_32x32x16_bf16 v[32:47], v[208:211], v[144:147], v[32:47]\n\t" \
  "v_exp_f32_e32 v101, v101\n\t" \
  "v_exp_f32_e32 v102, v102\n\t" \
  "v_exp_f32_e32 v103, v103\n\t" \
  "v_mfma_f32_32x32x16_bf16 v[48:63], v[212:215], v[144:147], v[48:63]\n\t" \
  "s_waitcnt lgkmcnt(0)\n\t" \
  "v_cmp_gt_u32_e32 vcc, 24, v236\n\t" \
  "s_cbranch_vccnz SPINB6r_%=\n\t" \
  "BACKB6r_%=:\n\t" \
  "v_exp_f32_e32 v104, v104\n\t" \
  "v_exp_f32_e32 v105, v105\n\t" \
  "v_mfma_f32_32x32x16_bf16 v[0:15], v[192:195], v[152:155], v[0:15]\n\t" \
  "ds_read_b128 v[176:179], v216 offset:0\n\t" \
  "v_exp_f32_e32 v106, v106\n\t" \
  "v_exp_f32_e32 v107, v107\n\t" \
  "v_mfma_f32_32x32x16_bf16 v[16:31], v[196:199], v[152:155], v[16:31]\n\t" \
  "ds_read_b128 v[180:183], v216 offset:8192\n\t" \
  "v_exp_f32_e32 v108, v108\n\t" \
  "v_exp_f32_e32 v109, v109\n\t" \
  "v_mfma_f32_32x32x16_bf16 v[32:47], v[200:203], v[152:155], v[32:47]\n\t" \
  "ds_read_b128 v[184:187], v217 offset:0\n\t" \
  "v_exp_f32_e32 v110, v110\n\t" \
  "v_exp_f32_e32 v111, v111\n\t" \
  "v_add_f32_e32 v231, v231, v96\n\t" \
  "v_mfma_f32_32x32x16_bf16 v[48:63], v[204:207], v[152:155], v[48:63]\n\t" \
  "ds_read_b128 v[188:191], v217 offset:8192\n\t" \
  "v_add_f32_e32 v232, v232, v97\n\t" \
  "v_add_f32_e32 v231, v231, v98\n\t" \
  "v_add_f32_e32 v232, v232, v99\n\t" \
  "v_mov_b32_e32 v237, 21\n\t" \
  "ds_write_b32 v229, v237\n\t" \
  "v_mul_f32_e32 v0, v0, v239\n\t" \
  "v_mul_f32_e32 v1, v1, v239\n\t" \
  "v_mul_f32_e32 v2, v2, v239\n\t" \
  "v_mul_f32_e32 v3, v3, v239\n\t" \
  "v_mul_f32_e32 v4, v4, v239\n\t" \
  "v_mul_f32_e32 v5, v5, v239\n\t" \
  "v_mul_f32_e32 v6, v6, v239\n\t" \
  "v_mul_f32_e32 v7, v7, v239\n\t" \
  "v_mul_f32_e32 v8, v8, v239\n\t" \
  "v_mul_f32_e32 v9, v9, v239\n\t" \
  "v_mul_f32_e32 v10, v10, v239\n\t" \
  "v_mul_f32_e32 v11, v11, v239\n\t" \
  "v_mul_f32_e32 v12, v12, v239\n\t" \
  "v_mul_f32_e32 v13, v13, v239\n\t" \
  "v_mul_f32_e32 v14, v14, v239\n\t" \
  "v_mul_f32_e32 v15, v15, v239\n\t" \
  "v_mul_f32_e32 v16, v16, v239\n\t" \
  "v_mul_f32_e32 v17, v17, v239\n\t" \
  "v_mul_f32_e32 v18, v18, v239\n\t" \
  "v_mul_f32_e32 v19, v19, v239\n\t" \
  "v_mul_f32_e32 v20, v20, v239\n\t" \
  "v_mul_f32_e32 v21, v21, v239\n\t" \
  "v_mul_f32_e32 v22, v22, v239\n\t" \
  "v_mul_f32_e32 v23, v23, v239\n\t" \
  "v_mul_f32_e32 v24, v24, v239\n\t" \
  "v_mul_f32_e32 v25, v25, v239\n\t" \
  "v_mul_f32_e32 v26, v26, v239\n\t" \
  "v_mul_f32_e32 v27, v27, v239\n\t" \
  "v_mul_f32_e32 v28, v28, v239\n\t" \
  "v_mul_f32_e32 v29, v29, v239\n\t" \
  "v_mul_f32_e32 v30, v30, v239\n\t" \
  "v_mul_f32_e32 v31, v31, v239\n\t" \
  "v_mul_f32_e32 v32, v32, v239\n\t" \
  "v_mul_f32_e32 v33, v33, v239\n\t" \
  "v_mul_f32_e32 v34, v34, v239\n\t" \
  "v_mul_f32_e32 v35, v35, v239\n\t" \
  "v_mul_f32_e32 v36, v36, v239\n\t" \
  "v_mul_f32_e32 v37, v37, v239\n\t" \
  "v_mul_f32_e32 v38, v38, v239\n\t" \
  "v_mul_f32_e32 v39, v39, v239\n\t" \
  "v_mul_f32_e32 v40, v40, v239\n\t" \
  "v_mul_f32_e32 v41, v41, v239\n\t" \
  "v_mul_f32_e32 v42, v42, v239\n\t" \
  "v_mul_f32_e32 v43, v43, v239\n\t" \
  "v_mul_f32_e32 v44, v44, v239\n\t" \
  "v_mul_f32_e32 v45, v45, v239\n\t" \
  "v_mul_f32_e32 v46, v46, v239\n\t" \
  "v_mul_f32_e32 v47, v47, v239\n\t" \
  "v_mul_f32_e32 v48, v48, v239\n\t" \
  "v_mul_f32_e32 v49, v49, v239\n\t" \
  "v_mul_f32_e32 v50, v50, v239\n\t" \
  "v_mul_f32_e32 v51, v51, v239\n\t" \
  "v_mul_f32_e32 v52, v52, v239\n\t" \
  "v_mul_f32_e32 v53, v53, v239\n\t" \
  "v_mul_f32_e32 v54, v54, v239\n\t" \
  "v_mul_f32_e32 v55, v55, v239\n\t" \
  "v_mul_f32_e32 v56, v56, v239\n\t" \
  "v_mul_f32_e32 v57, v57, v239\n\t" \
  "v_mul_f32_e32 v58, v58, v239\n\t" \
  "v_mul_f32_e32 v59, v59, v239\n\t" \
  "v_mul_f32_e32 v60, v60, v239\n\t" \
  "v_mul_f32_e32 v61, v61, v239\n\t" \
  "v_mul_f32_e32 v62, v62, v239\n\t" \
  "v_mul_f32_e32 v63, v63, v239\n\t" \
  "s_branch NEXT6_%=\n\t" \
  "SPINA7_%=:\n\t" \
  "s_mov_b32 %[ctr], 1048576\n\t" \
  "SPINLA7_%=:\n\t" \
  "ds_read_b32 v236, v228\n\t" \
  "s_sub_u32 %[ctr], %[ctr], 1\n\t" \
  "s_waitcnt lgkmcnt(0)\n\t" \
  "v_cmp_gt_u32_e32 vcc, 25, v236\n\t" \
  "s_cbranch_scc1 BACKA7_%=\n\t" \
  "s_cbranch_vccnz SPINLA7_%=\n\t" \
  "s_branch BACKA7_%=\n\t" \
  "RARE7_%=:\n\t" \
  "v_max_f32_e32 v238, 0, v233\n\t" \
  "v_add_f32_e32 v230, v230, v238\n\t" \
  "v_sub_f32_e32 v239, 0, v238\n\t" \
  "v_exp_f32_e32 v239, v239\n\t" \
  "v_sub_f32_e32 v128, v128, v238\n\t" \
  "v_sub_f32_e32 v129, v129, v238\n\t" \
  "v_sub_f32_e32 v130, v130, v238\n\t" \
  "v_sub_f32_e32 v131, v131, v238\n\t" \
  "v_sub_f32_e32 v132, v132, v238\n\t" \
  "v_sub_f32_e32 v133, v133, v238\n\t" \
  "v_sub_f32_e32 v134, v134, v238\n\t" \
  "v_sub_f32_e32 v135, v135, v238\n\t" \
  "v_sub_f32_e32 v136, v136, v238\n\t" \
  "v_sub_f32_e32 v137, v137, v238\n\t" \
  "v_sub_f32_e32 v138, v138, v238\n\t" \
  "v_sub_f32_e32 v139, v139, v238\n\t" \
  "v_sub_f32_e32 v140, v140, v238\n\t" \
  "v_sub_f32_e32 v141, v141, v238\n\t" \
  "v_sub_f32_e32 v142, v142, v238\n\t" \
  "v_sub_f32_e32 v143, v143, v238\n\t" \
  "v_sub_f32_e32 v144, v144, v238\n\t" \
  "v_sub_f32_e32 v145, v145, v238\n\t" \
  "v_sub_f32_e32 v146, v146, v238\n\t" \
  "v_sub_f32_e32 v147, v147, v238\n\t" \
  "v_sub_f32_e32 v148, v148, v238\n\t" \
  "v_sub_f32_e32 v149, v149, v238\n\t" \
  "v_sub_f32_e32 v150, v150, v238\n\t" \
  "v_sub_f32_e32 v151, v151, v238\n\t" \
  "v_sub_f32_e32 v152, v152, v238\n\t" \
  "v_sub_f32_e32 v153, v153, v238\n\t" \
  "v_sub_f32_e32 v154, v154, v238\n\t" \
  "v_sub_f32_e32 v155, v155, v238\n\t" \
  "v_sub_f32_e32 v156, v156, v238\n\t" \
  "v_sub_f32_e32 v157, v157, v238\n\t" \
  "v_sub_f32_e32 v158, v158, v238\n\t" \
  "v_sub_f32_e32 v159, v159, v238\n\t" \
  "v_sub_f32_e32 v160, 0, v230\n\t" \
  "v_sub_f32_e32 v161, 0, v230\n\t" \
  "v_sub_f32_e32 v162, 0, v230\n\t" \
  "v_sub_f32_e32 v163, 0, v230\n\t" \
  "v_sub_f32_e32 v164, 0, v230\n\t" \
  "v_sub_f32_e32 v165, 0, v230\n\t" \
  "v_sub_f32_e32 v166, 0, v230\n\t" \
  "v_sub_f32_e32 v167, 0, v230\n\t" \
  "v_sub_f32_e32 v168, 0, v230\n\t" \
  "v_sub_f32_e32 v169, 0, v230\n\t" \
  "v_sub_f32_e32 v170, 0, v230\n\t" \
  "v_sub_f32_e32 v171, 0, v230\n\t" \
  "v_sub_f32_e32 v172, 0, v230\n\t" \
  "v_sub_f32_e32 v173, 0, v230\n\t" \
  "v_sub_f32_e32 v174, 0, v230\n\t" \
  "v_sub_f32_e32 v175, 0, v230\n\t" \
  "v_mul_f32_e32 v231, v231, v239\n\t" \
  "v_mul_f32_e32 v232, v232, v239\n\t" \
  "v_mfma_f32_32x32x16_bf16 v[48:63], v[196:199], v[104:107], v[48:63]\n\t" \
  "ds_read_b64_tr_b16 v[196:197], v225 offset:28672\n\t" \
  "ds_read_b64_tr_b16 v[198:199], v225 offset:30720\n\t" \
  "v_exp_f32_e32 v128, v128\n\t" \
  "v_exp_f32_e32 v129, v129\n\t" \
  "s_waitcnt lgkmcnt(8)\n\t" \
  "v_mfma_f32_32x32x16_bf16 v[0:15], v[200:203], v[112:115], v[0:15]\n\t" \
  "ds_read_b64_tr_b16 v[200:201], v226 offset:28672\n\t" \
  "ds_read_b64_tr_b16 v[202:203], v226 offset:30720\n\t" \
  "v_exp_f32_e32 v130, v130\n\t" \
  "v_exp_f32_e32 v131, v131\n\t" \
  "v_mfma_f32_32x32x16_bf16 v[16:31], v[204:207], v[112:115], v[16:31]\n\t" \
  "ds_read_b64_tr_b16 v[204:205], v227 offset:28672\n\t" \
  "ds_read_b64_tr_b16 v[206:207], v227 offset:30720\n\t" \
  "v_exp_f32_e32 v132, v132\n\t" \
  "v_exp_f32_e32 v133, v133\n\t" \
  "s_waitcnt lgkmcnt(8)\n\t" \
  "v_mfma_f32_32x32x16_bf16 v[32:47], v[208:211], v[112:115], v[32:47]\n\t" \
  "v_exp_f32_e32 v134, v134\n\t" \
  "v_exp_f32_e32 v135, v135\n\t" \
  "v_exp_f32_e32 v136, v136\n\t" \
  "v_mfma_f32_32x32x16_bf16 v[48:63], v[212:215], v[112:115], v[48:63]\n\t" \
  "v_exp_f32_e32 v137, v137\n\t" \
  "v_exp_f32_e32 v138, v138\n\t" \
  "v_exp_f32_e32 v139, v139\n\t" \
  "s_waitcnt lgkmcnt(4)\n\t" \
  "v_mfma_f32_32x32x16_bf16 v[0:15], v[192:195], v[120:123], v[0:15]\n\t" \
  "v_exp_f32_e32 v140, v140\n\t" \
  "v_exp_f32_e32 v141, v141\n\t" \
  "v_exp_f32_e32 v142, v142\n\t" \
  "v_mfma_f32_32x32x16_bf16 v[16:31], v[196:199], v[120:123], v[16:31]\n\t" \
  "v_exp_f32_e32 v143, v143\n\t" \
  "s_waitcnt lgkmcnt(0)\n\t" \
  "v_mfma_f32_32x32x16_bf16 v[32:47], v[200:203], v[120:123], v[32:47]\n\t" \
  "v_add_f32_e32 v231, v231, v128\n\t" \
  "v_add_f32_e32 v232, v232, v129\n\t" \
  "v_add_f32_e32 v231, v231, v130\n\t" \
  "v_add_f32_e32 v232, v232, v131\n\t" \
  "v_mfma_f32_32x32x16_bf16 v[48:63], v[204:207], v[120:123], v[48:63]\n\t" \
  "v_mov_b32_e32 v237, 23\n\t" \
  "ds_write_b32 v229, v237\n\t" \
  "v_mul_f32_e32 v0, v0, v239\n\t" \
  "v_mul_f32_e32 v1, v1, v239\n\t" \
  "v_mul_f32_e32 v2, v2, v239\n\t" \
  "v_mul_f32_e32 v3, v3, v239\n\t" \
  "v_mul_f32_e32 v4, v4, v239\n\t" \
  "v_mul_f32_e32 v5, v5, v239\n\t" \
  "v_mul_f32_e32 v6, v6, v239\n\t" \
  "v_mul_f32_e32 v7, v7, v239\n\t" \
  "v_mul_f32_e32 v8, v8, v239\n\t" \
  "v_mul_f32_e32 v9, v9, v239\n\t" \
  "v_mul_f32_e32 v10, v10, v239\n\t" \
  "v_mul_f32_e32 v11, v11, v239\n\t" \
  "v_mul_f32_e32 v12, v12, v239\n\t" \
  "v_mul_f32_e32 v13, v13, v239\n\t" \
  "v_mul_f32_e32 v14, v14, v239\n\t" \
  "v_mul_f32_e32 v15, v15, v239\n\t" \
  "v_mul_f32_e32 v16, v16, v239\n\t" \
  "v_mul_f32_e32 v17, v17, v239\n\t" \
  "v_mul_f32_e32 v18, v18, v239\n\t" \
  "v_mul_f32_e32 v19, v19, v239\n\t" \
  "v_mul_f32_e32 v20, v20, v239\n\t" \
  "v_mul_f32_e32 v21, v21, v239\n\t" \
  "v_mul_f32_e32 v22, v22, v239\n\t" \
  "v_mul_f32_e32 v23, v23, v239\n\t" \
  "v_mul_f32_e32 v24, v24, v239\n\t" \
  "v_mul_f32_e32 v25, v25, v239\n\t" \
  "v_mul_f32_e32 v26, v26, v239\n\t" \
  "v_mul_f32_e32 v27, v27, v239\n\t" \
  "v_mul_f32_e32 v28, v28, v239\n\t" \
  "v_mul_f32_e32 v29, v29, v239\n\t" \
  "v_mul_f32_e32 v30, v30, v239\n\t" \
  "v_mul_f32_e32 v31, v31, v239\n\t" \
  "v_mul_f32_e32 v32, v32, v239\n\t" \
  "v_mul_f32_e32 v33, v33, v239\n\t" \
  "v_mul_f32_e32 v34, v34, v239\n\t" \
  "v_mul_f32_e32 v35, v35, v239\n\t" \
  "v_mul_f32_e32 v36, v36, v239\n\t" \
  "v_mul_f32_e32 v37, v37, v239\n\t" \
  "v_mul_f32_e32 v38, v38, v239\n\t" \
  "v_mul_f32_e32 v39, v39, v239\n\t" \
  "v_mul_f32_e32 v40, v40, v239\n\t" \
  "v_mul_f32_e32 v41, v41, v239\n\t" \
  "v_mul_f32_e32 v42, v42, v239\n\t" \
  "v_mul_f32_e32 v43, v43, v239\n\t" \
  "v_mul_f32_e32 v44, v44, v239\n\t" \
  "v_mul_f32_e32 v45, v45, v239\n\t" \
  "v_mul_f32_e32 v46, v46, v239\n\t" \
  "v_mul_f32_e32 v47, v47, v239\n\t" \
  "v_mul_f32_e32 v48, v48, v239\n\t" \
  "v_mul_f32_e32 v49, v49, v239\n\t" \
  "v_mul_f32_e32 v50, v50, v239\n\t" \
  "v_mul_f32_e32 v51, v51, v239\n\t" \
  "v_mul_f32_e32 v52, v52, v239\n\t" \
  "v_mul_f32_e32 v53, v53, v239\n\t" \
  "v_mul_f32_e32 v54, v54, v239\n\t" \
  "v_mul_f32_e32 v55, v55, v239\n\t" \
  "v_mul_f32_e32 v56, v56, v239\n\t" \
  "v_mul_f32_e32 v57, v57, v239\n\t" \
  "v_mul_f32_e32 v58, v58, v239\n\t" \
  "v_mul_f32_e32 v59, v59, v239\n\t" \
  "v_mul_f32_e32 v60, v60, v239\n\t" \
  "v_mul_f32_e32 v61, v61, v239\n\t" \
  "v_mul_f32_e32 v62, v62, v239\n\t" \
  "v_mul_f32_e32 v63, v63, v239\n\t" \
  "s_branch NEXT7_%=\n\t" \
  "SPIND_%=:\n\t" \
  "s_mov_b32 %[ctr], 1048576\n\t" \
  "SPINLD_%=:\n\t" \
  "ds_read_b32 v236, v228\n\t" \
  "s_sub_u32 %[ctr], %[ctr], 1\n\t" \
  "s_waitcnt lgkmcnt(0)\n\t" \
  "v_cmp_gt_u32_e32 vcc, 26, v236\n\t" \
  "s_cbranch_scc1 BACKD_%=\n\t" \
  "s_cbranch_vccnz SPINLD_%=\n\t" \
  "s_branch BACKD_%=\n\t" \
  "OWN_%=:\n\t" \
  "ds_read_b64_tr_b16 v[192:193], v224 offset:32768\n\t" \
  "ds_read_b64_tr_b16 v[194:195], v224 offset:34816\n\t" \
  "ds_read_b64_tr_b16 v[196:197], v225 offset:32768\n\t" \
  "ds_read_b64_tr_b16 v[198:199], v225 offset:34816\n\t" \
  "ds_read_b64_tr_b16 v[200:201], v226 offset:32768\n\t" \
  "ds_read_b64_tr_b16 v[202:203], v226 offset:34816\n\t" \
  "ds_read_b64_tr_b16 v[204:205], v227 offset:32768\n\t" \
  "ds_read_b64_tr_b16 v[206:207], v227 offset:34816\n\t" \
  "ds_read_b64_tr_b16 v[208:209], v224 offset:36864\n\t" \
  "ds_read_b64_tr_b16 v[210:211], v224 offset:38912\n\t" \
  "ds_read_b64_tr_b16 v[212:213], v225 offset:36864\n\t" \
  "ds_read_b64_tr_b16 v[214:215], v225 offset:38912\n\t" \
  "v_cvt_pk_bf16_f32 v128, v128, v129\n\t" \
  "v_add_f32_e32 v231, v231, v132\n\t" \
  "v_cvt_pk_bf16_f32 v129, v130, v131\n\t" \
  "v_add_f32_e32 v232, v232, v133\n\t" \
  "v_cvt_pk_bf16_f32 v130, v132, v133\n\t" \
  "v_add_f32_e32 v231, v231, v134\n\t" \
  "v_cvt_pk_bf16_f32 v131, v134, v135\n\t" \
  "v_add_f32_e32 v232, v232, v135\n\t" \
  "s_waitcnt lgkmcnt(8)\n\t" \
  "v_mfma_f32_32x32x16_bf16 v[0:15], v[192:195], v[128:131], v[0:15]\n\t" \
  "ds_read_b64_tr_b16 v[192:193], v226 offset:36864\n\t" \
  "ds_read_b64_tr_b16 v[194:195], v226 offset:38912\n\t" \
  "v_add_f32_e32 v231, v231, v136\n\t" \
  "v_add_f32_e32 v232, v232, v137\n\t" \
  "v_add_f32_e32 v231, v231, v138\n\t" \
  "v_add_f32_e32 v232, v232, v139\n\t" \
  "v_mfma_f32_32x32x16_bf16 v[16:31], v[196:199], v[128:131], v[16:31]\n\t" \
  "ds_read_b64_tr_b16 v[196:197], v227 offset:36864\n\t" \
  "ds_read_b64_tr_b16 v[198:199], v227 offset:38912\n\t" \
  "v_cvt_pk_bf16_f32 v136, v136, v137\n\t" \
  "v_add_f32_e32 v231, v231, v140\n\t" \
  "v_cvt_pk_bf16_f32 v137, v138, v139\n\t" \
  "v_add_f32_e32 v232, v232, v141\n\t" \
  "s_waitcnt lgkmcnt(8)\n\t" \
  "v_mfma_f32_32x32x16_bf16 v[32:47], v[200:203], v[128:131], v[32:47]\n\t" \
  "ds_read_b64_tr_b16 v[200:201], v224 offset:40960\n\t" \
  "ds_read_b64_tr_b16 v[202:203], v224 offset:43008\n\t" \
  "v_cvt_pk_bf16_f32 v138, v140, v141\n\t" \
  "v_add_f32_e32 v231, v231, v142\n\t" \
  "v_cvt_pk_bf16_f32 v139, v142, v143\n\t" \
  "v_mfma_f32_32x32x16_bf16 v[48:63], v[204:207], v[128:131], v[48:63]\n\t" \
  "ds_read_b64_tr_b16 v[204:205], v224 offset:45056\n\t" \
  "ds_read_b64_tr_b16 v[206:207], v224 offset:47104\n\t" \
  "v_exp_f32_e32 v144, v144\n\t" \
  "v_exp_f32_e32 v145, v145\n\t" \
  "s_waitcnt lgkmcnt(8)\n\t" \
  "v_mfma_f32_32x32x16_bf16 v[0:15], v[208:211], v[136:139], v[0:15]\n\t" \
  "ds_read_b64_tr_b16 v[208:209], v225 offset:40960\n\t" \
  "ds_read_b64_tr_b16 v[210:211], v225 offset:43008\n\t" \
  "v_exp_f32_e32 v146, v146\n\t" \
  "v_exp_f32_e32 v147, v147\n\t" \
  "v_mfma_f32_32x32x16_bf16 v[16:31], v[212:215], v[136:139], v[16:31]\n\t" \
  "ds_read_b64_tr_b16 v[212:213], v225 offset:45056\n\t" \
  "ds_read_b64_tr_b16 v[214:215], v225 offset:47104\n\t" \
  "v_exp_f32_e32 v148, v148\n\t" \
  "v_exp_f32_e32 v149, v149\n\t" \
  "s_waitcnt lgkmcnt(8)\n\t" \
  "v_mfma_f32_32x32x16_bf16 v[32:47], v[192:195], v[136:139], v[32:47]\n\t" \
  "ds_read_b64_tr_b16 v[192:193], v226 offset:40960\n\t" \
  "ds_read_b64_tr_b16 v[194:195], v226 offset:43008\n\t" \
  "v_exp_f32_e32 v150, v150\n\t" \
  "v_exp_f32_e32 v151, v151\n\t" \
  "v_mfma_f32_32x32x16_bf16 v[48:63], v[196:199], v[136:139], v[48:63]\n\t" \
  "ds_read_b64_tr_b16 v[196:197], v226 offset:45056\n\t" \
  "ds_read_b64_tr_b16 v[198:199], v226 offset:47104\n\t" \
  "v_add_f32_e32 v232, v232, v143\n\t" \
  "v_exp_f32_e32 v152, v152\n\t" \
  "v_exp_f32_e32 v153, v153\n\t" \
  "v_exp_f32_e32 v154, v154\n\t" \
  "v_exp_f32_e32 v155, v155\n\t" \
  "v_exp_f32_e32 v156, v156\n\t" \
  "v_exp_f32_e32 v157, v157\n\t" \
  "v_exp_f32_e32 v158, v158\n\t" \
  "v_exp_f32_e32 v159, v159\n\t" \
  "v_add_f32_e32 v231, v231, v144\n\t" \
  "v_add_f32_e32 v232, v232, v145\n\t" \
  "v_add_f32_e32 v231, v231, v146\n\t" \
  "v_add_f32_e32 v232, v232, v147\n\t" \
  "v_cvt_pk_bf16_f32 v144, v144, v145\n\t" \
  "v_add_f32_e32 v231, v231, v148\n\t" \
  "v_cvt_pk_bf16_f32 v145, v146, v147\n\t" \
  "v_add_f32_e32 v232, v232, v149\n\t" \
  "v_cvt_pk_bf16_f32 v146, v148, v149\n\t" \
  "v_add_f32_e32 v231, v231, v150\n\t" \
  "v_cvt_pk_bf16_f32 v147, v150, v151\n\t" \
  "s_waitcnt lgkmcnt(8)\n\t" \
  "s_nop 0\n\t" \
  "v_mfma_f32_32x32x16_bf16 v[0:15], v[200:203], v[144:147], v[0:15]\n\t" \
  "ds_read_b64_tr_b16 v[200:201], v227 offset:40960\n\t" \
  "ds_read_b64_tr_b16 v[202:203], v227 offset:43008\n\t" \
  "v_add_f32_e32 v232, v232, v151\n\t" \
  "v_add_f32_e32 v231, v231, v152\n\t" \
  "v_add_f32_e32 v232, v232, v153\n\t" \
  "v_add_f32_e32 v231, v231, v154\n\t" \
  "v_add_f32_e32 v232, v232, v155\n\t" \
  "v_cvt_pk_bf16_f32 v152, v152, v153\n\t" \
  "v_add_f32_e32 v231, v231, v156\n\t" \
  "v_cvt_pk_bf16_f32 v153, v154, v155\n\t" \
  "v_add_f32_e32 v232, v232, v157\n\t" \
  "v_cvt_pk_bf16_f32 v154, v156, v157\n\t" \
  "v_add_f32_e32 v231, v231, v158\n\t" \
  "v_cvt_pk_bf16_f32 v155, v158, v159\n\t" \
  "s_nop 1\n\t" \
  "v_mfma_f32_32x32x16_bf16 v[0:15], v[204:207], v[152:155], v[0:15]\n\t" \
  "ds_read_b64_tr_b16 v[204:205], v227 offset:45056\n\t" \
  "ds_read_b64_tr_b16 v[206:207], v227 offset:47104\n\t" \
  "v_add_f32_e32 v232, v232, v159\n\t" \
  "v_add_f32_e32 v231, v231, v232\n\t" \
  "v_mov_b32_e32 v235, v231\n\t" \
  "s_nop 1\n\t" \
  "v_permlane32_swap_b32_e32 v231, v235\n\t" \
  "v_add_f32_e32 v231, v231, v235\n\t" \
  "v_rcp_f32_e32 v238, v231\n\t" \
  "v_log_f32_e32 v239, v231\n\t" \
  "s_nop 0\n\t" \
  "v_add_f32_e32 v239, v239, v230\n\t" \
  "ds_write_b32 v242, v239\n\t" \
  "s_waitcnt lgkmcnt(9)\n\t" \
  "v_mfma_f32_32x32x16_bf16 v[16:31], v[208:211], v[144:147], v[16:31]\n\t" \
  "v_mul_f32_e32 v0, v0, v238\n\t" \
  "v_mul_f32_e32 v1, v1, v238\n\t" \
  "v_mul_f32_e32 v2, v2, v238\n\t" \
  "v_mul_f32_e32 v3, v3, v238\n\t" \
  "v_mul_f32_e32 v4, v4, v238\n\t" \
  "v_mul_f32_e32 v5, v5, v238\n\t" \
  "v_mul_f32_e32 v6, v6, v238\n\t" \
  "v_mul_f32_e32 v7, v7, v238\n\t" \
  "v_mul_f32_e32 v8, v8, v238\n\t" \
  "v_mul_f32_e32 v9, v9, v238\n\t" \
  "v_mul_f32_e32 v10, v10, v238\n\t" \
  "v_mul_f32_e32 v11, v11, v238\n\t" \
  "v_mul_f32_e32 v12, v12, v238\n\t" \
  "v_mul_f32_e32 v13, v13, v238\n\t" \
  "v_mfma_f32_32x32x16_bf16 v[16:31], v[212:215], v[152:155], v[16:31]\n\t" \
  "v_mul_f32_e32 v14, v14, v238\n\t" \
  "v_mul_f32_e32 v15, v15, v238\n\t" \
  "v_cvt_pk_bf16_f32 v0, v0, v1\n\t" \
  "v_cvt_pk_bf16_f32 v1, v2, v3\n\t" \
  "v_cvt_pk_bf16_f32 v2, v4, v5\n\t" \
  "v_cvt_pk_bf16_f32 v3, v6, v7\n\t" \
  "ds_write_b128 v240, v[0:3] offset:0\n\t" \
  "v_cvt_pk_bf16_f32 v8, v8, v9\n\t" \
  "v_cvt_pk_bf16_f32 v9, v10, v11\n\t" \
  "v_cvt_pk_bf16_f32 v10, v12, v13\n\t" \
  "v_cvt_pk_bf16_f32 v11, v14, v15\n\t" \
  "ds_write_b128 v240, v[8:11] offset:1024\n\t" \
  "v_mul_f32_e32 v16, v16, v238\n\t" \
  "v_mul_f32_e32 v17, v17, v238\n\t" \
  "s_waitcnt lgkmcnt(7)\n\t" \
  "v_mfma_f32_32x32x16_bf16 v[32:47], v[192:195], v[144:147], v[32:47]\n\t" \
  "v_mul_f32_e32 v18, v18, v238\n\t" \
  "v_mul_f32_e32 v19, v19, v238\n\t" \
  "v_mul_f32_e32 v20, v20, v238\n\t" \
  "v_mul_f32_e32 v21, v21, v238\n\t" \
  "v_mul_f32_e32 v22, v22, v238\n\t" \
  "v_mul_f32_e32 v23, v23, v238\n\t" \
  "v_mul_f32_e32 v24, v24, v238\n\t" \
  "v_mul_f32_e32 v25, v25, v238\n\t" \
  "v_mul_f32_e32 v26, v26, v238\n\t" \
  "v_mul_f32_e32 v27, v27, v238\n\t" \
  "v_mul_f32_e32 v28, v28, v238\n\t" \
  "v_mul_f32_e32 v29, v29, v238\n\t" \
  "v_mul_f32_e32 v30, v30, v238\n\t" \
  "v_mul_f32_e32 v31, v31, v238\n\t" \
  "v_mfma_f32_32x32x16_bf16 v[32:47], v[196:199], v[152:155], v[32:47]\n\t" \
  "v_cvt_pk_bf16_f32 v16, v16, v17\n\t" \
  "v_cvt_pk_bf16_f32 v17, v18, v19\n\t" \
  "v_cvt_pk_bf16_f32 v18, v20, v21\n\t" \
  "v_cvt_pk_bf16_f32 v19, v22, v23\n\t" \
  "ds_write_b128 v240, v[16:19] offset:2048\n\t" \
  "v_cvt_pk_bf16_f32 v24, v24, v25\n\t" \
  "v_cvt_pk_bf16_f32 v25, v26, v27\n\t" \
  "v_cvt_pk_bf16_f32 v26, v28, v29\n\t" \
  "v_cvt_pk_bf16_f32 v27, v30, v31\n\t" \
  "ds_write_b128 v240, v[24:27] offset:3072\n\t" \
  "s_nop 1\n\t" \
  "v_mul_f32_e32 v32, v32, v238\n\t" \
  "v_mul_f32_e32 v33, v33, v238\n\t" \
  "v_mul_f32_e32 v34, v34, v238\n\t" \
  "v_mul_f32_e32 v35, v35, v238\n\t" \
  "s_waitcnt lgkmcnt(5)\n\t" \
  "v_mfma_f32_32x32x16_bf16 v[48:63], v[200:203], v[144:147], v[48:63]\n\t" \
  "v_mul_f32_e32 v36, v36, v238\n\t" \
  "v_mul_f32_e32 v37, v37, v238\n\t" \
  "v_mul_f32_e32 v38, v38, v238\n\t" \
  "v_mul_f32_e32 v39, v39, v238\n\t" \
  "v_mul_f32_e32 v40, v40, v238\n\t" \
  "v_mul_f32_e32 v41, v41, v238\n\t" \
  "v_mul_f32_e32 v42, v42, v238\n\t" \
  "v_mul_f32_e32 v43, v43, v238\n\t" \
  "v_mul_f32_e32 v44, v44, v238\n\t" \
  "v_mul_f32_e32 v45, v45, v238\n\t" \
  "v_mul_f32_e32 v46, v46, v238\n\t" \
  "v_mul_f32_e32 v47, v47, v238\n\t" \
  "v_cvt_pk_bf16_f32 v32, v32, v33\n\t" \
  "v_cvt_pk_bf16_f32 v33, v34, v35\n\t" \
  "v_mfma_f32_32x32x16_bf16 v[48:63], v[204:207], v[152:155], v[48:63]\n\t" \
  "v_cvt_pk_bf16_f32 v34, v36, v37\n\t" \
  "v_cvt_pk_bf16_f32 v35, v38, v39\n\t" \
  "ds_write_b128 v241, v[32:35] offset:0\n\t" \
  "v_cvt_pk_bf16_f32 v40, v40, v41\n\t" \
  "v_cvt_pk_bf16_f32 v41, v42, v43\n\t" \
  "v_cvt_pk_bf16_f32 v42, v44, v45\n\t" \
  "v_cvt_pk_bf16_f32 v43, v46, v47\n\t" \
  "ds_write_b128 v241, v[40:43] offset:1024\n\t" \
  "s_nop 3\n\t" \
  "v_mul_f32_e32 v48, v48, v238\n\t" \
  "v_mul_f32_e32 v49, v49, v238\n\t" \
  "v_mul_f32_e32 v50, v50, v238\n\t" \
  "v_mul_f32_e32 v51, v51, v238\n\t" \
  "v_mul_f32_e32 v52, v52, v238\n\t" \
  "v_mul_f32_e32 v53, v53, v238\n\t" \
  "v_mul_f32_e32 v54, v54, v238\n\t" \
  "v_mul_f32_e32 v55, v55, v238\n\t" \
  "v_mul_f32_e32 v56, v56, v238\n\t" \
  "v_mul_f32_e32 v57, v57, v238\n\t" \
  "v_mul_f32_e32 v58, v58, v238\n\t" \
  "v_mul_f32_e32 v59, v59, v238\n\t" \
  "v_mul_f32_e32 v60, v60, v238\n\t" \
  "v_mul_f32_e32 v61, v61, v238\n\t" \
  "v_mul_f32_e32 v62, v62, v238\n\t" \
  "v_mul_f32_e32 v63, v63, v238\n\t" \
  "v_cvt_pk_bf16_f32 v48, v48, v49\n\t" \
  "v_cvt_pk_bf16_f32 v49, v50, v51\n\t" \
  "v_cvt_pk_bf16_f32 v50, v52, v53\n\t" \
  "v_cvt_pk_bf16_f32 v51, v54, v55\n\t" \
  "ds_write_b128 v241, v[48:51] offset:2048\n\t" \
  "v_cvt_pk_bf16_f32 v56, v56, v57\n\t" \
  "v_cvt_pk_bf16_f32 v57, v58, v59\n\t" \
  "v_cvt_pk_bf16_f32 v58, v60, v61\n\t" \
  "v_cvt_pk_bf16_f32 v59, v62, v63\n\t" \
  "ds_write_b128 v241, v[56:59] offset:3072\n\t" \
  "v_mov_b32_e32 v237, 25\n\t" \
  "ds_write_b32 v229, v237\n\t" \
  "s_waitcnt lgkmcnt(0)\n\t" \
  "s_branch FIN_%=\n\t" \
  "END_%=:\n\t" \
  ""
#define MAIN_CLOBBERS "v0", "v1", "v2", "v3", "v4", "v5", "v6", "v7", "v8", "v9", "v10", "v11", "v12", "v13", "v14", "v15", "v16", "v17", "v18", "v19", "v20", "v21", "v22", "v23", "v24", "v25", "v26", "v27", "v28", "v29", "v30", "v31", "v32", "v33", "v34", "v35", "v36", "v37", "v38", "v39", "v40", "v41", "v42", "v43", "v44", "v45", "v46", "v47", "v48", "v49", "v50", "v51", "v52", "v53", "v54", "v55", "v56", "v57", "v58", "v59", "v60", "v61", "v62", "v63", "v96", "v97", "v98", "v99", "v100", "v101", "v102", "v103", "v104", "v105", "v106", "v107", "v108", "v109", "v110", "v111", "v112", "v113", "v114", "v115", "v116", "v117", "v118", "v119", "v120", "v121", "v122", "v123", "v124", "v125", "v126", "v127", "v128", "v129", "v130", "v131", "v132", "v133", "v134", "v135", "v136", "v137", "v138", "v139", "v140", "v141", "v142", "v143", "v144", "v145", "v146", "v147", "v148", "v149", "v150", "v151", "v152", "v153", "v154", "v155", "v156", "v157", "v158", "v159", "v160", "v161", "v162", "v163", "v164", "v165", "v166", "v167", "v168", "v169", "v170", "v171", "v172", "v173", "v174", "v175", "v176", "v177", "v178", "v179", "v180", "v181", "v182", "v183", "v184", "v185", "v186", "v187", "v188", "v189", "v190", "v191", "v192", "v193", "v194", "v195", "v196", "v197", "v198", "v199", "v200", "v201", "v202", "v203", "v204", "v205", "v206", "v207", "v208", "v209", "v210", "v211", "v212", "v213", "v214", "v215", "v230", "v231", "v232", "v233", "v234", "v235", "v236", "v237", "v238", "v239", "v244", "v245", "v246", "v247"

#define DI __device__ __forceinline__
#define MFMA32(a, b, c) __builtin_amdgcn_mfma_f32_32x32x16_bf16((a), (b), (c), 0, 0, 0)
#define RLX __ATOMIC_RELAXED, __HIP_MEMORY_SCOPE_AGENT
constexpr int SK = 2048;
constexpr float CSC = 0.088388347648318447f * 1.4426950408889634f;
constexpr unsigned SPIN_CAP = 1u << 18;
constexpr int AUX_SC1 = 16;
constexpr int CODE_KB = 27;
constexpr int LD_DW = 3, LD_D = 3, LD_DO = 2;
constexpr int PAUSE_I = 13;

constexpr unsigned FSTR = 64, FQ_WG = 4 * FSTR, F2_WG = 32 * FSTR;
constexpr unsigned WS_EP = 0, WS_QX = 1u << 20, WS_LSE = 3u << 20, WS_IN1 = 4u << 20, WS_IN2 = 12u << 20;
constexpr unsigned WS_FQ = 21u << 20, WS_F1 = WS_FQ + 256 * FQ_WG, WS_F2 = WS_F1 + 256 * FQ_WG, WS_END = 24u << 20;
constexpr unsigned L_B = 65536, L_C0 = 131072, L_C1 = 147456, L_FL = 162560, L_TOT = 163840;
constexpr unsigned FL_FULL = L_FL, FL_QS = L_FL + 64, FL_X1 = L_FL + 128, FL_PROG = L_FL + 256;
constexpr unsigned L_OWN = L_C1, L_OWNLSE = L_C1 + 8192;
constexpr unsigned slotA(int i) { return 16384u * i; }
constexpr unsigned slotB(int i) { return L_B + 16384u * i; }
constexpr unsigned woslot(int i) { return i == 0 ? slotA(1) : i == 1 ? slotB(3) : i == 2 ? slotA(2) : i == 3 ? L_C0 : i == 4 ? slotA(3) : i == 5 ? slotB(0) : i == 6 ? slotA(0) : slotB(1); }
constexpr unsigned L_OF = slotB(2);

DI unsigned rfl(unsigned x) { return __builtin_amdgcn_readfirstlane(x); }
DI u64 uniform64(u64 v) { return ((u64)rfl((unsigned)(v >> 32)) << 32) | rfl((unsigned)v); }
DI unsigned cvtpk(float lo, float hi) { f32x2 v = {lo, hi}; bf16x2 b = __builtin_convertvector(v, bf16x2); return __builtin_bit_cast(unsigned, b); }
DI bf16x4 trread(unsigned addr, int off) { bf16x4 r; asm volatile("ds_read_b64_tr_b16 %0, %1 offset:%2" : "=v"(r) : "v"(addr), "i"(off) : "memory"); return r; }
DI unsigned lane_id() { unsigned l; asm volatile("v_mbcnt_lo_u32_b32 %0, -1, 0\n\tv_mbcnt_hi_u32_b32 %0, -1, %0" : "=v"(l)); return l; }
DI bool lds_ge(unsigned addr, unsigned need) { unsigned v; asm volatile("ds_read_b32 %0, %1\n\ts_waitcnt lgkmcnt(0)" : "=v"(v) : "v"(addr) : "memory"); return __all(v >= need); }
DI void lds_wait_ge(unsigned addr, unsigned need) {
#pragma unroll 1
  for (unsigned it = 0; it < SPIN_CAP * 16; ++it) { if (lds_ge(addr, need)) break; }
}
DI void lds_wait_ge_s(unsigned addr, unsigned need) {
#pragma unroll 1
  for (unsigned it = 0; it < SPIN_CAP * 16; ++it) { if (lds_ge(addr, need)) break; __builtin_amdgcn_s_sleep(1); }
}
DI void lds_post(unsigned addr, unsigned val) { asm volatile("ds_write_b32 %0, %1" :: "v"(addr), "v"(val) : "memory"); }
DI void wait_flags(const gu64* p, u64 tag) {
#pragma unroll 1
  for (unsigned it = 0; it < SPIN_CAP; ++it) {
    const u64 v = __hip_atomic_load(p, RLX);
    if (__all(v == tag)) break;
    __builtin_amdgcn_s_sleep(2);
  }
}
template <bool NT> DI void dma4(unsigned m0, unsigned v0, unsigned v1, unsigned v2, unsigned v3, __amdgpu_buffer_rsrc_t srd, unsigned s0, unsigned s1, unsigned s2, unsigned s3) {
  if constexpr (NT)
    asm volatile("s_mov_b32 m0, %0\n\ts_nop 0\n\tbuffer_load_dwordx4 %1, %5, %6 offen nt lds\n\tbuffer_load_dwordx4 %2, %5, %7 offen offset:1024 nt lds\n\t"
                 "buffer_load_dwordx4 %3, %5, %8 offen offset:2048 nt lds\n\tbuffer_load_dwordx4 %4, %5, %9 offen offset:3072 nt lds"
                 :: "s"(m0), "v"(v0), "v"(v1), "v"(v2), "v"(v3), "s"(srd), "s"(s0), "s"(s1), "s"(s2), "s"(s3) : "memory");
  else
    asm volatile("s_mov_b32 m0, %0\n\ts_nop 0\n\tbuffer_load_dwordx4 %1, %5, %6 offen lds\n\tbuffer_load_dwordx4 %2, %5, %7 offen offset:1024 lds\n\t"
                 "buffer_load_dwordx4 %3, %5, %8 offen offset:2048 lds\n\tbuffer_load_dwordx4 %4, %5, %9 offen offset:3072 lds"
                 :: "s"(m0), "v"(v0), "v"(v1), "v"(v2), "v"(v3), "s"(srd), "s"(s0), "s"(s1), "s"(s2), "s"(s3) : "memory");
}

struct Ld { unsigned ldsb, L, fullA, progA, qseenA; unsigned vK[4], vV, vW, vO, vX[4]; __amdgpu_buffer_rsrc_t sX, sW, sK, sV, sO; int pub; };
template <int N> DI void vmwait() { asm volatile("s_waitcnt vmcnt(%0)" :: "i"(N) : "memory"); }
DI void ld_pub(Ld& c, int T) { c.pub = T; lds_post(c.fullA, T); }
DI void ld_drain(Ld& c, int I) {
  if (c.pub < I - 3) { vmwait<12>(); ld_pub(c, I - 3); }
  if (c.pub < I - 2) { vmwait<8>(); ld_pub(c, I - 2); }
  if (c.pub < I - 1) { vmwait<4>(); ld_pub(c, I - 1); }
  if (c.pub < I) { vmwait<0>(); ld_pub(c, I); }
}
DI void ld_slot(Ld& c, int I, unsigned need) { if (!lds_ge(c.progA, need)) { ld_drain(c, I); lds_wait_ge_s(c.progA, need); } }
template <int D> DI void ld_after(Ld& c, int I) { if (c.pub < I - D + 1) { vmwait<4 * D>(); ld_pub(c, I - D + 1); } }
DI void ld_x(Ld& c, unsigned I) {
  const unsigned L = c.L, so = (16u * (L & 1u)) * 1024u + 256u * (2 * I + (L >> 1));
  dma4<false>(rfl(c.ldsb + (I == 0 ? L_C0 : slotB(3)) + 4096u * L), c.vX[0], c.vX[1], c.vX[2], c.vX[3], c.sX, rfl(so), rfl(so + 3072u), rfl(so + 6144u), rfl(so + 9216u));
}
DI unsigned wslot(unsigned j) { return j == 7 ? 0u : (j & 1u) * L_B + (j >> 1) * 16384u; }
DI void ld_w(Ld& c, unsigned j) {
  const unsigned so = (64u * j + 16u * c.L) * 8192u;
  dma4<false>(rfl(c.ldsb + wslot(j) + 4096u * c.L), c.vW, c.vW, c.vW, c.vW, c.sW, rfl(so), rfl(so + 31744u), rfl(so + 63488u), rfl(so + 95232u));
}
DI void ld_run(Ld& c) {
#pragma unroll 1
  for (int I = 3; I < 10; ++I) {
    if (I == 9) ld_slot(c, I, 1);
    ld_w(c, I - 2);
    ld_after<LD_DW>(c, I);
  }

#pragma unroll 1
  for (int J = 0; J < 16; ++J) {
    const int I = 10 + J;
    const unsigned t = 2u * (J >> 2) + (J & 1u), need = (unsigned)(0xFDECB8A986874253ull >> (4 * J)) & 15u;
    if (I == PAUSE_I) { ld_drain(c, I); lds_wait_ge_s(c.qseenA, 1); }
    ld_slot(c, I, need);
    const unsigned so = rfl(16384u * t + 4096u * c.L);
    if (((J >> 1) & 1) == 0) dma4<true>(rfl(c.ldsb + 16384u * ((t + 1) & 3u) + 4096u * c.L), c.vK[0], c.vK[1], c.vK[2], c.vK[3], c.sK, so, so, so, so);
    else dma4<true>(rfl(c.ldsb + (t == 4 ? L_C0 : L_B + 16384u * (t < 4 ? t : t - 5)) + 4096u * c.L), c.vV, c.vV, c.vV, c.vV, c.sV, so, so, so, so);
    ld_after<LD_D>(c, I);
  }
#pragma unroll 1
  for (int i = 0; i < 8; ++i) {
    const int I = 26 + i;
    ld_slot(c, I, 16 + i);
    const unsigned so = (64u * (i & 1u) + 16u * c.L) * 1024u + 256u * (i >> 1), slot = ((0x50438271u >> (4 * i)) & 15u) * 16384u;
    dma4<false>(rfl(c.ldsb + slot + 4096u * c.L), c.vO, c.vO, c.vO, c.vO, c.sO, rfl(so), rfl(so + 3072u), rfl(so + 6144u), rfl(so + 9216u));
    ld_after<LD_DO>(c, I);
  }
}
static_assert(woslot(0) == 1 * 16384 && woslot(1) == 7 * 16384 && woslot(2) == 2 * 16384 && woslot(3) == 8 * 16384 && woslot(4) == 3 * 16384 && woslot(5) == 4 * 16384 && woslot(6) == 0 && woslot(7) == 5 * 16384, "wo slot table");

__global__ __launch_bounds__(512, 2) void gqa_fused(const u16* __restrict__ x, const u16* __restrict__ K, const u16* __restrict__ V, const u16* __restrict__ wq,
                                                    const u16* __restrict__ wo, u16* __restrict__ out, char* __restrict__ ws) {
  extern __shared__ __attribute__((aligned(16))) char lds[];
  const unsigned tid = threadIdx.x, wave = rfl(tid >> 6);
  const unsigned bid = blockIdx.x, g = bid & 7u, s = (bid >> 3) & 3u, b = bid >> 5;
  const unsigned grp = b * 8 + g, wgi = grp * 4 + s, head = 4 * g + s;
  const unsigned ldsb = (unsigned)(size_t)lds;
  u64 pc0; asm volatile("s_getpc_b64 %0" : "=s"(pc0));
  asm volatile("" :: "s"(x), "s"(K), "s"(V), "s"(wq), "s"(wo), "s"(out), "s"(ws));
  const __amdgpu_buffer_rsrc_t sWS = __builtin_amdgcn_make_buffer_rsrc((void*)ws, (short)0, WS_END, 0x00020000);
  gu64* epp = (gu64*)(ws + WS_EP + b * 256);
  u64 E;
  if (__builtin_expect(wave >= 4, 1)) {
    const unsigned lane = lane_id();
    Ld c; c.pub = 0;
    c.ldsb = ldsb; c.L = wave - 4; c.fullA = ldsb + FL_FULL + 4 * c.L; c.progA = ldsb + FL_PROG + 256 * (lane & 3u); c.qseenA = ldsb + FL_QS + 4 * (lane & 3u);
    const unsigned i4 = lane >> 4, c15 = lane & 15u;
#pragma unroll
    for (int q = 0; q < 4; ++q) c.vX[q] = 1024u * i4 + 16u * (c15 ^ (4u * q + i4));
    c.vW = 8192u * i4 + 16u * (c15 ^ (i4 << 2));
    c.sX = __builtin_amdgcn_make_buffer_rsrc((void*)((const char*)x + (size_t)b * 32768), (short)0, 32768, 0x00020000);
    c.sW = __builtin_amdgcn_make_buffer_rsrc((void*)((const char*)wq + head * 256), (short)0, (4u << 20) - head * 256, 0x00020000);
    ld_x(c, 0); ld_x(c, 1); ld_w(c, 0);
    asm volatile("s_barrier" ::: "memory");
#pragma unroll
    for (int q = 0; q < 4; ++q) c.vK[q] = 256u * i4 + 16u * (c15 ^ (4u * q + i4));
    c.vV = 256u * i4 + 16u * (c15 ^ (i4 << 2)); c.vO = 1024u * i4 + 16u * (c15 ^ (i4 << 2));
    c.sK = __builtin_amdgcn_make_buffer_rsrc((void*)((const char*)K + ((size_t)grp * SK + s * 512) * 256), (short)0, 131072, 0x00020000);
    c.sV = __builtin_amdgcn_make_buffer_rsrc((void*)((const char*)V + ((size_t)grp * SK + s * 512) * 256), (short)0, 131072, 0x00020000);
    c.sO = __builtin_amdgcn_make_buffer_rsrc((void*)((const char*)wo + (size_t)head * 131072), (short)0, 131072, 0x00020000);
    ld_run(c);
    const u64 Ev = __hip_atomic_load(epp, RLX);
    ld_drain(c, 34);
    E = uniform64(Ev);
  } else {
    const unsigned w = wave;
    if ((bid >> 3) * 4 + w < CODE_KB) {
      const __amdgpu_buffer_rsrc_t sC = __builtin_amdgcn_make_buffer_rsrc((void*)(pc0 & ~255ull), (short)0, CODE_KB * 1024, 0x00020000);
      asm volatile("s_mov_b32 m0, %0\n\ts_nop 0\n\tbuffer_load_dwordx4 %1, %2, %3 offen lds" :: "s"(rfl(ldsb + L_OWN + 1024u * w)), "v"(lane_id() * 16u), "s"(sC), "s"(rfl(((bid >> 3) * 4 + w) * 1024u)) : "memory");
    }
    if (w == 3) {
      const unsigned ln = lane_id();
      const unsigned off = ln < 32 ? WS_F2 + wgi * F2_WG + ln * FSTR : ln < 36 ? WS_F1 + wgi * FQ_WG + (ln - 32) * FSTR : WS_FQ + wgi * FQ_WG + (ln & 3u) * FSTR;
      asm volatile("s_mov_b32 m0, %0\n\ts_nop 0\n\tbuffer_load_dword %1, %2, 0 offen sc1 lds" :: "s"(rfl(ldsb + L_OWN + 4096u)), "v"(off), "s"(sWS) : "memory");
    }
    { const unsigned t4 = tid * 4u; *(unsigned*)(lds + L_FL + t4) = 0u; if (tid < 64) *(unsigned*)(lds + L_FL + 1024u + t4) = 0u; }
    asm volatile("s_waitcnt lgkmcnt(0)\n\ts_barrier" ::: "memory");
    const u64 Ev = __hip_atomic_load(epp, RLX);
    {
      const unsigned lane = lane_id(), r = lane & 31u, hh = lane >> 5;
      const unsigned fullA = ldsb + FL_FULL + 4 * (lane & 3u), progA = ldsb + FL_PROG + 256 * w + 4 * lane;
      const unsigned q4 = (lane >> 2) & 3u, p4 = lane & 3u, blk = (lane >> 4) & 1u;
      unsigned ka[4];
#pragma unroll
      for (int i = 0; i < 4; ++i) ka[i] = 256u * r + 16u * ((2u * i + hh) ^ (r & 15u));
      const unsigned wa = ldsb + 2048u * hh + 256u * q4 + 64u * (w ^ q4) + 32u * blk + 8u * p4;
      f32x16 acc = {};
#pragma unroll
      for (int j = 0; j < 8; ++j) {
        lds_wait_ge(fullA, 3 + j);
        const unsigned a = wa + wslot(j);
        bf16x4 lo0 = trread(a, 0), hi0 = trread(a, 1024), lo1 = trread(a, 4096), hi1 = trread(a, 5120);
        bf16x4 lo2 = trread(a, 8192), hi2 = trread(a, 9216), lo3 = trread(a, 12288), hi3 = trread(a, 13312);
        bf16x8 xb[4];
        { const unsigned xo = ((j & 4u) ? slotB(3) : L_C0) + 8192u * ((j >> 1) & 1u), fl = (j & 1u) << 7;
#pragma unroll
          for (int ks = 0; ks < 4; ++ks) xb[ks] = *(const bf16x8*)(lds + xo + (ka[ks] ^ fl)); }
        asm volatile("s_waitcnt lgkmcnt(0)" : "+v"(lo0), "+v"(hi0), "+v"(lo1), "+v"(hi1), "+v"(lo2), "+v"(hi2), "+v"(lo3), "+v"(hi3), "+v"(xb[0]), "+v"(xb[1]), "+v"(xb[2]), "+v"(xb[3]) :: "memory");
        lds_post(progA, j + 1);
        __builtin_amdgcn_sched_barrier(0);
        acc = MFMA32(__builtin_shufflevector(lo0, hi0, 0, 1, 2, 3, 4, 5, 6, 7), xb[0], acc);
        acc = MFMA32(__builtin_shufflevector(lo1, hi1, 0, 1, 2, 3, 4, 5, 6, 7), xb[1], acc);
        acc = MFMA32(__builtin_shufflevector(lo2, hi2, 0, 1, 2, 3, 4, 5, 6, 7), xb[2], acc);
        acc = MFMA32(__builtin_shufflevector(lo3, hi3, 0, 1, 2, 3, 4, 5, 6, 7), xb[3], acc);
      }
      E = uniform64(Ev);
#pragma unroll
      for (int k2 = 0; k2 < 2; ++k2) {
        unsigned a0 = cvtpk(acc[8 * k2 + 0] * CSC, acc[8 * k2 + 1] * CSC), a1 = cvtpk(acc[8 * k2 + 2] * CSC, acc[8 * k2 + 3] * CSC);
        unsigned b0 = cvtpk(acc[8 * k2 + 4] * CSC, acc[8 * k2 + 5] * CSC), b1 = cvtpk(acc[8 * k2 + 6] * CSC, acc[8 * k2 + 7] * CSC);
        auto r0 = __builtin_amdgcn_permlane32_swap(a0, b0, false, false); auto r1 = __builtin_amdgcn_permlane32_swap(a1, b1, false, false);
        __builtin_amdgcn_raw_buffer_store_b128(u32x4{r0[0], r1[0], r0[1], r1[1]}, sWS, WS_QX + wgi * 8192u + (2 * w + k2) * 1024u + lane * 16u, 0, AUX_SC1);
      }
      asm volatile("s_waitcnt vmcnt(0)" ::: "memory");
      if (lane == 0) __hip_atomic_store((gu64*)(ws + WS_FQ + wgi * FQ_WG + w * FSTR), E + 1, RLX);
    }
    const unsigned lane = lane_id(), r = lane & 31u, hh = lane >> 5;
    const unsigned q4 = (lane >> 2) & 3u, p4 = lane & 3u, blk = (lane >> 4) & 1u;
    wait_flags((const gu64*)(ws + WS_FQ + (grp * 4 + w) * FQ_WG + (lane & 3u) * FSTR), E + 1);
    lds_post(ldsb + FL_QS + 4 * w, 1);
    u32x16 qa, qb;
    {
      u32x4 qf[8];
#pragma unroll
      for (int i = 0; i < 8; ++i) qf[i] = __builtin_amdgcn_raw_buffer_load_b128(sWS, WS_QX + (grp * 4 + w) * 8192u + i * 1024u + lane * 16u, 0, AUX_SC1);
#pragma unroll
      for (int i = 0; i < 4; ++i)
#pragma unroll
        for (int e = 0; e < 4; ++e) { qa[4 * i + e] = qf[i][e]; qb[4 * i + e] = qf[4 + i][e]; }
    }
    {
      u32x8 kav; u32x4 vav, eo; u32x2 fp;
#pragma unroll
      for (int i = 0; i < 8; ++i) kav[i] = ldsb + 256u * r + 16u * ((2u * i + hh) ^ (r & 15u));
#pragma unroll
      for (int db = 0; db < 4; ++db) vav[db] = ldsb + L_B + 256u * (4u * hh + q4) + 64u * ((unsigned)db ^ q4) + 32u * blk + 8u * p4;
      fp[0] = ldsb + FL_FULL + 4 * (lane & 3u); fp[1] = ldsb + FL_PROG + 256 * w + 4 * lane;
      const unsigned dst = (grp * 4 + w) * 4 + s, own = w == s;
      eo[0] = (own ? ldsb + L_OWN : WS_IN1 + dst * 8192u) + lane * 16u; eo[1] = eo[0] + 4096u;
      eo[2] = (own ? ldsb + L_OWNLSE : WS_LSE + dst * 256u) + lane * 4u; eo[3] = 0;
      asm volatile("s_waitcnt vmcnt(0)" : "+v"(qa), "+v"(qb) :: "memory");
      unsigned ctr;
      asm volatile(MAIN_ASM
                   : [ctr] "=&s"(ctr)
                   : "{v[64:79]}"(qa), "{v[80:95]}"(qb), "{v[216:223]}"(kav), "{v[224:227]}"(vav), "{v[228:229]}"(fp), "{v[240:243]}"(eo), [srd] "s"(sWS), [own] "s"(rfl(own))
                   : MAIN_CLOBBERS, "vcc", "scc", "memory");
    }
    if (w != s) {
      asm volatile("s_waitcnt vmcnt(0)" ::: "memory");
      if (lane_id() == 0) __hip_atomic_store((gu64*)(ws + WS_F1 + (grp * 4 + w) * FQ_WG + s * FSTR), E + 2, RLX);
    }
  }
  const unsigned v = wave;
  bf16x4 alo[2][2][4], ahi[2][2][4];
  {
    const unsigned lane = lane_id(), hh = lane >> 5;
    const unsigned q4 = (lane >> 2) & 3u, p4 = lane & 3u, blk = (lane >> 4) & 1u;
    lds_wait_ge_s(ldsb + FL_FULL + 4 * (lane & 3u), 34);
    const unsigned nq = v >> 1;
    const unsigned sl0 = nq == 0 ? woslot(0) : nq == 1 ? woslot(2) : nq == 2 ? woslot(4) : woslot(6);
    const unsigned sl1 = nq == 0 ? woslot(1) : nq == 1 ? woslot(3) : nq == 2 ? woslot(5) : woslot(7);
#pragma unroll
    for (int nb = 0; nb < 2; ++nb) {
      const unsigned dbp = 2u * (v & 1u) + nb;
      const unsigned base = ldsb + 256u * (4u * hh + q4) + 64u * (dbp ^ q4) + 32u * blk + 8u * p4;
#pragma unroll
      for (int kh = 0; kh < 2; ++kh) {
        const unsigned a = base + (kh ? sl1 : sl0);
#pragma unroll
        for (int ks = 0; ks < 4; ++ks) { alo[nb][kh][ks] = trread(a, 4096 * ks); ahi[nb][kh][ks] = trread(a, 4096 * ks + 2048); }
        asm volatile("s_waitcnt lgkmcnt(0)" : "+v"(alo[nb][kh][0]), "+v"(ahi[nb][kh][0]), "+v"(alo[nb][kh][1]), "+v"(ahi[nb][kh][1]), "+v"(alo[nb][kh][2]), "+v"(ahi[nb][kh][2]), "+v"(alo[nb][kh][3]), "+v"(ahi[nb][kh][3]) :: "memory");
      }
    }
  }
  {
    const unsigned lane = lane_id();
    u32x4 pv[3]; unsigned ls[3];
    const unsigned s1 = (s + 1) & 3u, s2 = (s + 2) & 3u, s3 = (s + 3) & 3u;
    if (v == 4) { const unsigned l3 = lane & 3u, src = l3 == 3 ? s1 : (s + 1 + l3) & 3u;
      wait_flags((const gu64*)(ws + WS_F1 + wgi * FQ_WG + src * FSTR), E + 2); lds_post(ldsb + FL_X1, 1); }
    else lds_wait_ge(ldsb + FL_X1, 1);
    pv[0] = __builtin_amdgcn_raw_buffer_load_b128(sWS, WS_IN1 + (wgi * 4 + s1) * 8192u + v * 1024u + lane * 16u, 0, AUX_SC1);
    pv[1] = __builtin_amdgcn_raw_buffer_load_b128(sWS, WS_IN1 + (wgi * 4 + s2) * 8192u + v * 1024u + lane * 16u, 0, AUX_SC1);
    pv[2] = __builtin_amdgcn_raw_buffer_load_b128(sWS, WS_IN1 + (wgi * 4 + s3) * 8192u + v * 1024u + lane * 16u, 0, AUX_SC1);
    ls[0] = __builtin_amdgcn_raw_buffer_load_b32(sWS, WS_LSE + (wgi * 4 + s1) * 256u + lane * 4u, 0, AUX_SC1);
    ls[1] = __builtin_amdgcn_raw_buffer_load_b32(sWS, WS_LSE + (wgi * 4 + s2) * 256u + lane * 4u, 0, AUX_SC1);
    ls[2] = __builtin_amdgcn_raw_buffer_load_b32(sWS, WS_LSE + (wgi * 4 + s3) * 256u + lane * 4u, 0, AUX_SC1);
    asm volatile("s_waitcnt vmcnt(0)" : "+v"(pv[0]), "+v"(pv[1]), "+v"(pv[2]), "+v"(ls[0]), "+v"(ls[1]), "+v"(ls[2]) :: "memory");
    asm volatile("s_waitcnt lgkmcnt(0)\n\ts_barrier" ::: "memory");
    const u32x4 po = *(const u32x4*)(lds + L_OWN + v * 1024u + lane * 16u);
    float e0 = __uint_as_float(*(const unsigned*)(lds + L_OWNLSE + lane * 4u)), e1 = __uint_as_float(ls[0]), e2 = __uint_as_float(ls[1]), e3 = __uint_as_float(ls[2]);
    const float M = fmaxf(fmaxf(e0, e1), fmaxf(e2, e3));
    e0 = __builtin_amdgcn_exp2f(e0 - M); e1 = __builtin_amdgcn_exp2f(e1 - M); e2 = __builtin_amdgcn_exp2f(e2 - M); e3 = __builtin_amdgcn_exp2f(e3 - M);
    const float iv = __builtin_amdgcn_rcpf(e0 + e1 + e2 + e3);
    e0 *= iv; e1 *= iv; e2 *= iv; e3 *= iv;
    u32x4 of;
#pragma unroll
    for (int d = 0; d < 4; ++d) {
      const float lo = e0 * __uint_as_float(po[d] << 16) + e1 * __uint_as_float(pv[0][d] << 16) + e2 * __uint_as_float(pv[1][d] << 16) + e3 * __uint_as_float(pv[2][d] << 16);
      const float hi = e0 * __uint_as_float(po[d] & 0xffff0000u) + e1 * __uint_as_float(pv[0][d] & 0xffff0000u) + e2 * __uint_as_float(pv[1][d] & 0xffff0000u) + e3 * __uint_as_float(pv[2][d] & 0xffff0000u);
      of[d] = cvtpk(lo, hi);
    }
    *(u32x4*)(lds + L_OF + v * 1024u + lane * 16u) = of;
  }
  asm volatile("s_waitcnt lgkmcnt(0)\n\ts_barrier" ::: "memory");
  {
    const unsigned lane = lane_id();
    bf16x8 ob[8];
#pragma unroll
    for (int f = 0; f < 8; ++f) ob[f] = *(const bf16x8*)(lds + L_OF + f * 1024u + lane * 16u);
    f32x16 acc[2] = {};
#pragma unroll
    for (int k2 = 0; k2 < 2; ++k2)
#pragma unroll
      for (int ks = 0; ks < 4; ++ks)
#pragma unroll
        for (int nb = 0; nb < 2; ++nb)
          acc[nb] = MFMA32(__builtin_shufflevector(alo[nb][k2][ks], ahi[nb][k2][ks], 0, 1, 2, 3, 4, 5, 6, 7), ob[4 * k2 + ks], acc[nb]);
#pragma unroll
    for (int nb = 0; nb < 2; ++nb)
#pragma unroll
      for (int u = 0; u < 2; ++u) {
        const u32x4 f = {cvtpk(acc[nb][8 * u], acc[nb][8 * u + 1]), cvtpk(acc[nb][8 * u + 2], acc[nb][8 * u + 3]), cvtpk(acc[nb][8 * u + 4], acc[nb][8 * u + 5]), cvtpk(acc[nb][8 * u + 6], acc[nb][8 * u + 7])};
        const unsigned j = 4 * v + 2 * nb + u;
        __builtin_amdgcn_raw_buffer_store_b128(f, sWS, WS_IN2 + ((b * 32 + j) * 32 + head) * 1024u + lane * 16u, 0, AUX_SC1);
      }
    asm volatile("s_waitcnt vmcnt(0)" ::: "memory");
    if (lane < 4) __hip_atomic_store((gu64*)(ws + WS_F2 + (b * 32 + 4 * v + lane) * F2_WG + head * FSTR), E + 3, RLX);
  }
  {
    const unsigned lane = lane_id();
    u32x4 pv[4];
    wait_flags((const gu64*)(ws + WS_F2 + wgi * F2_WG + (4 * v + (lane & 3u)) * FSTR), E + 3);
#pragma unroll
    for (int i = 0; i < 4; ++i) pv[i] = __builtin_amdgcn_raw_buffer_load_b128(sWS, WS_IN2 + (wgi * 32 + 4 * v + i) * 1024u + lane * 16u, 0, AUX_SC1);
    asm volatile("s_waitcnt vmcnt(0)" : "+v"(pv[0]), "+v"(pv[1]), "+v"(pv[2]), "+v"(pv[3]) :: "memory");
    float* red = (float*)(lds + (v < 4 ? L_OWN + v * 2048u : L_OF + 8192u + (v - 4) * 2048u));
#pragma unroll
    for (int d = 0; d < 4; ++d) {
      const float lo = __uint_as_float(pv[0][d] << 16) + __uint_as_float(pv[1][d] << 16) + __uint_as_float(pv[2][d] << 16) + __uint_as_float(pv[3][d] << 16);
      const float hi = __uint_as_float(pv[0][d] & 0xffff0000u) + __uint_as_float(pv[1][d] & 0xffff0000u) + __uint_as_float(pv[2][d] & 0xffff0000u) + __uint_as_float(pv[3][d] & 0xffff0000u);
      red[(2 * d) * 64 + lane] = lo; red[(2 * d + 1) * 64 + lane] = hi;
    }
  }
  asm volatile("s_waitcnt lgkmcnt(0)\n\ts_barrier" ::: "memory");
  if (wave < 4) {
    const unsigned lane = lane_id(), r = lane & 31u, hh = lane >> 5;
    const unsigned ep = wave;
    float lo = 0.f, hi = 0.f;
#pragma unroll
    for (int i = 0; i < 8; ++i) {
      const float* red = (const float*)(lds + (i < 4 ? L_OWN + i * 2048u : L_OF + 8192u + (i - 4) * 2048u));
      lo += red[(2 * ep) * 64 + lane]; hi += red[(2 * ep + 1) * 64 + lane];
    }
    const unsigned col = 16 * head + 8 * (ep >> 1) + 4 * hh + 2 * (ep & 1u);
    *(unsigned*)(out + ((size_t)b * 32 + r) * 512 + col) = cvtpk(lo, hi);
  }
  if (g == 0 && s == 0 && wave == 0 && lane_id() == 0) __hip_atomic_store(epp, E + 16, RLX);
}

extern "C" void kernel_launch(void* const* d_in, const int* in_sizes, int n_in, void* d_out, int out_size, void* d_ws, size_t ws_size, hipStream_t stream) {
  (void)in_sizes; (void)n_in; (void)out_size; (void)ws_size;
  (void)hipFuncSetAttribute((const void*)gqa_fused, hipFuncAttributeMaxDynamicSharedMemorySize, L_TOT);
  gqa_fused<<<256, 512, L_TOT, stream>>>((const u16*)d_in[0], (const u16*)d_in[1], (const u16*)d_in[2], (const u16*)d_in[3], (const u16*)d_in[4], (u16*)d_out, (char*)d_ws);
}
```
